```python
import torch

def _softmax(x, axis=-1):
    return torch.softmax(x, dim=axis)
def _gelu(x):
    return torch.nn.functional.gelu(x, approximate="tanh")
def _silu(x):
    return torch.nn.functional.silu(x)
def _sigmoid(x):
    return torch.sigmoid(x)
def _layernorm(x, axis=-1, eps=1e-5):
    m = x.mean(dim=axis, keepdim=True)
    v = x.var(dim=axis, keepdim=True, unbiased=False)
    return (x - m) / torch.sqrt(v + eps)
def _scatter_add(tgt, idx, src, axis=0):
    return tgt.clone().index_add_(axis, idx, src)
def _segment_sum(src, seg, n):
    out = torch.zeros((n,) + src.shape[1:], dtype=src.dtype)
    return out.index_add_(0, seg, src)
def _pad(x, pw):
    flat = tuple(v for pair in reversed(pw) for v in pair)
    return torch.nn.functional.pad(x, flat)

kernel_name = 'standalone__kv_cache_quant_int4_int2_kv_with_per_channel_or_per_token_scales__sampling'

def setup_inputs():

    _w = (lambda a: (lambda t: t.to(torch.bfloat16) if t.is_floating_point() else t)(torch.as_tensor(a)))
    B = 8
    Sq = 4
    Skv = 4096
    Hq = 32
    Hkv = 32
    Dh = 192
    bits = 2
    G = 64
    if G <= 0 or G >= Skv:
        G = max(1, Skv // 2)
    Skv = (Skv // G) * G
    ngk = Skv // G
    qmax = (1 << bits) - 1
    return {
        "q": _w((lambda *s: torch.randn(*s, dtype=torch.bfloat16) * 3.2)(B, Hq, Sq, Dh)),
        "qk": _w(torch.randint(0, qmax + 1, (B, Hkv, Skv, Dh)).to(torch.int32)),
        "qv": _w(torch.randint(0, qmax + 1, (B, Hkv, Skv, Dh)).to(torch.int32)),
        "k_scales": _w(torch.abs((lambda *s: torch.randn(*s, dtype=torch.bfloat16) * 3.2)(B, Hkv, ngk, Dh)) * 0.05 + 0.02),
        "k_zeros": _w((lambda *s: torch.randn(*s, dtype=torch.bfloat16) * 3.2)(B, Hkv, ngk, Dh) + float(qmax) / 2.0),
        "v_scales": _w(torch.abs((lambda *s: torch.randn(*s, dtype=torch.bfloat16) * 0.05)(B, Hkv, Skv, 1)) * 0.05 + 0.02),
        "v_zeros": _w((lambda *s: torch.randn(*s, dtype=torch.bfloat16) * 0.05)(B, Hkv, Skv, 1) + float(qmax) / 2.0),
    }

def reference(q, qk, qv, k_scales, k_zeros, v_scales, v_zeros):
    B, Hq, Sq, Dh = q.shape
    Hkv, Skv = qk.shape[1], qk.shape[2]
    ngk = k_scales.shape[2]
    G = Skv // ngk
    ks = (lambda x, n, axis: torch.repeat_interleave(x, n, dim=axis))(k_scales, G, axis=2)
    kz = (lambda x, n, axis: torch.repeat_interleave(x, n, dim=axis))(k_zeros, G, axis=2)
    k = ((lambda x, dt: x.to(dt))(qk, q.dtype) - kz) * ks
    v = ((lambda x, dt: x.to(dt))(qv, q.dtype) - v_zeros) * v_scales
    rep = Hq // Hkv
    if rep > 1:
        k = (lambda x, n, axis: torch.repeat_interleave(x, n, dim=axis))(k, rep, axis=1)
        v = (lambda x, n, axis: torch.repeat_interleave(x, n, dim=axis))(v, rep, axis=1)
    scale = float(Dh) ** -0.5
    logits = torch.einsum("bhqd,bhkd->bhqk", q, k) * scale
    p = _softmax(logits, axis=-1)
    o = torch.einsum("bhqk,bhkd->bhqd", p, v)
    return o
```

```cpp
#include <hip/hip_runtime.h>
#include <hip/hip_bf16.h>
#include <stdint.h>

namespace {

constexpr int B_ = 8, H_ = 32, SQ = 4, SKV = 4096, DH = 192, GRP = 64, NGK = SKV / GRP;
constexpr int NWG = B_ * H_;
constexpr int NWAVE = 8, NTHR = NWAVE * 64;
constexpr int NST = 43;
constexpr int NQ = NWAVE * NST;
constexpr int NS = 3 * NST;
constexpr int NPR = (NS + 1) / 2;
constexpr int NROW = 22;
constexpr unsigned TBYTES = (unsigned)NWG * SKV * DH * 4u;
constexpr int RS = 6;
#define SLIP 2
#define SLM 5
#define TAU 788
#define ORG0 285
#define GLATE 17
#define SHM 18
#define LD 14
#define NSP 3
#define XTK 3
#define XFL 1
#define FORCE 0
constexpr int XROW = NROW - 1;
constexpr int MROW = 3 * NROW + XROW;
constexpr int SD = 20;
constexpr int PGS = 8, PLANE = 544;
constexpr int CK0 = 12, CK1 = 46, PCK = 31, PDLY = 3;
constexpr int SPAN0 = 30, SPAN = 16, RDN = 8, RUP = 20;
constexpr int WS_CTL = 1 << 18;
constexpr int WS_PART = 1 << 19;
constexpr int WS_PACE = 4 << 20;
constexpr int WS_X = 12 << 20;
constexpr int WS_BYTES = WS_X + (NWG / 2) * 32768;
constexpr int AUX_NT = 2, AUX_SC1 = 16;
constexpr int OSL = SQ * DH * 4;

using u32x4  = __attribute__((ext_vector_type(4))) unsigned;
using u32x2  = __attribute__((ext_vector_type(2))) unsigned;
using i32x4  = __attribute__((ext_vector_type(4))) int;
using f32x4  = __attribute__((ext_vector_type(4))) float;
using bf16x8 = __attribute__((ext_vector_type(8))) short;
using bf16x4 = __attribute__((ext_vector_type(4))) short;

using f32x2  = __attribute__((ext_vector_type(2))) float;
using bfx2   = __attribute__((ext_vector_type(2))) __bf16;
__device__ __forceinline__ unsigned cvtpk(float lo, float hi) {
  f32x2 f = {lo, hi};
  return __builtin_bit_cast(unsigned, __builtin_convertvector(f, bfx2));
}
__device__ __forceinline__ float bflo(unsigned p) { return __uint_as_float(p << 16); }
__device__ __forceinline__ float bfhi(unsigned p) { return __uint_as_float(p & 0xffff0000u); }
__device__ __forceinline__ float bfround(float x) { return bflo(cvtpk(x, x)); }

__device__ __forceinline__ u32x4 ldnt4(const unsigned short* p) { return __builtin_nontemporal_load(reinterpret_cast<const u32x4*>(p)); }

__device__ __forceinline__ i32x4 ldbuf(__amdgpu_buffer_rsrc_t r, int voff, unsigned soff) {
  return __builtin_bit_cast(i32x4, __builtin_amdgcn_raw_buffer_load_b128(r, voff, (int)soff, AUX_NT));
}

__device__ __forceinline__ unsigned deq2(int c0, int c1, unsigned z, unsigned s) {
  unsigned t = cvtpk((float)c0 - bflo(z), (float)c1 - bfhi(z));
  return cvtpk(bflo(t) * bflo(s), bfhi(t) * bfhi(s));
}

__device__ __forceinline__ unsigned lds_addr(int* p) { return (unsigned)(size_t)(__attribute__((address_space(3))) int*)p; }
__device__ __forceinline__ void lds_add(int* p, int v) {
  asm volatile("s_mov_b64 exec, 1\n\tds_add_u32 %0, %1\n\ts_mov_b64 exec, -1" :: "v"(lds_addr(p)), "v"(v) : "memory");
}
__device__ __forceinline__ void lds_max(int* p, int v) {
  asm volatile("s_mov_b64 exec, 1\n\tds_max_i32 %0, %1\n\ts_mov_b64 exec, -1" :: "v"(lds_addr(p)), "v"(v) : "memory");
}

template <bool MAX> __device__ __forceinline__ float rows(float x) {
  const auto a = __builtin_amdgcn_permlane16_swap(__float_as_uint(x), __float_as_uint(x), false, false);
  x = MAX ? fmaxf(__uint_as_float(a[0]), __uint_as_float(a[1])) : __uint_as_float(a[0]) + __uint_as_float(a[1]);
  const auto b = __builtin_amdgcn_permlane32_swap(__float_as_uint(x), __float_as_uint(x), false, false);
  return MAX ? fmaxf(__uint_as_float(b[0]), __uint_as_float(b[1])) : __uint_as_float(b[0]) + __uint_as_float(b[1]);
}

template <int N> __device__ __forceinline__ void wait_tv(unsigned tg, int d) {
  asm volatile("s_cmp_lt_i32 %0, 1\n\ts_cbranch_scc1 2f\n1:\n\ts_memrealtime vcc\n\ts_waitcnt lgkmcnt(0)\n\ts_sub_i32 %0, %1, vcc_lo\n\t"
               "s_cmp_gt_i32 %0, 0\n\ts_cbranch_scc1 1b\n2:\n\ts_waitcnt vmcnt(%2)" : "+s"(d) : "s"(tg), "n"(N) : "memory", "scc", "vcc");
}

__device__ __forceinline__ void lds_barrier() { asm volatile("s_waitcnt lgkmcnt(0)\n\ts_barrier" ::: "memory"); }

__device__ __forceinline__ int lds_ld(const int* p) {
  return __builtin_amdgcn_readfirstlane(__hip_atomic_load(p, __ATOMIC_RELAXED, __HIP_MEMORY_SCOPE_WORKGROUP));
}

__device__ __forceinline__ int load_time(const void* q) {
  unsigned long long t0, t1; int x;
  const unsigned long long p = (unsigned)__builtin_amdgcn_readfirstlane((unsigned)(size_t)q) | ((unsigned long long)(unsigned)__builtin_amdgcn_readfirstlane((unsigned)((size_t)q >> 32)) << 32);
  asm volatile("s_memtime %0\n\ts_waitcnt lgkmcnt(0)\n\ts_load_dword %2, %3, 0x0\n\ts_waitcnt lgkmcnt(0)\n\ts_memtime %1\n\ts_waitcnt lgkmcnt(0)"
               : "=&s"(t0), "=&s"(t1), "=&s"(x) : "s"(p) : "memory");
  return (int)(t1 - t0);
}

__device__ __forceinline__ unsigned pick(bool c, unsigned a, unsigned b) { return c ? a : b; }

template <int N> struct IC { static constexpr int value = N; };

__global__ __launch_bounds__(NTHR + 64, 3)
void kivi_attn_kernel(const __hip_bfloat16* __restrict__ q_, const int* __restrict__ qk, const int* __restrict__ qv,
                      const __hip_bfloat16* __restrict__ ks_, const __hip_bfloat16* __restrict__ kz_,
                      const __hip_bfloat16* __restrict__ vs_, const __hip_bfloat16* __restrict__ vz_,
                      __hip_bfloat16* __restrict__ out, unsigned char* ws) {
  __shared__ __attribute__((aligned(16))) unsigned char kst[NWAVE * OSL];
  __shared__ __attribute__((aligned(16))) unsigned short pl[NWAVE * NS * 16];
  __shared__ __attribute__((aligned(16))) unsigned kzs[MROW * DH];
  __shared__ __attribute__((aligned(16))) unsigned vzs[MROW * GRP];
  __shared__ __attribute__((aligned(16))) unsigned short tab[(NS + NST) * NWAVE * 4];
  __shared__ __attribute__((aligned(16))) unsigned short qs[3 * SQ * DH];
  __shared__ __attribute__((aligned(16))) float stats[4 * NWAVE * 8];
  __shared__ __attribute__((aligned(16))) float xmax[NWAVE * 4];
  __shared__ int cnt[4], rdy[1], pprog[1], mrg[1], xfl[1];
  __shared__ __attribute__((aligned(8))) int org[4];

  const int wg = (blockIdx.x & ~7) | ((blockIdx.x & 3) << 1) | ((blockIdx.x >> 2) & 1);
  const int tid = threadIdx.x, lane = tid & 63;
  const int w = __builtin_amdgcn_readfirstlane(tid >> 6);
  const int m = lane & 15, g = lane >> 4;
  if (tid == 0) {
    rdy[0] = 0; pprog[0] = 0; cnt[0] = 0; cnt[1] = 0; cnt[2] = 0; cnt[3] = 0; mrg[0] = 0; xfl[0] = 0;
    org[0] = ORG0 << 8; org[1] = 1; org[2] = (int)(unsigned)__builtin_amdgcn_s_memrealtime();
  }
  lds_barrier();

  const int ps = wg % SHM / 2 == 3 ? 0 : ((wg + NWG) % SHM / 2 == 3 ? 1 : ((wg + 2 * NWG) % SHM / 2 == 3 ? 2 : 3));
  const int l0 = ps == 0 ? LD : 0;
  const int r2 = 2 * NROW + (ps < 2 ? XROW : 0);
  auto frow = [=](int p) { return p == 2 ? r2 : ((p == 0) == (ps == 0) ? NROW : 0); };
  auto lrow = [=](int p) { return frow(p) - (p == ps ? 0 : (p + wg) % 3 * (NROW - 1)); };
  auto gbase = [=](int p) { return (p * NWG + wg) / 3 * NGK - lrow(p); };
  const int gba = gbase(ps == 0), gbb = gbase(ps != 0), gb2 = gbase(2);
  auto sc_off = [=](int v, int RL) { return (unsigned)(8 * v + RL * (gba + (8 * v >= NROW * RL ? gbb - gba : 0) + (8 * v >= r2 * RL ? gb2 - gbb : 0))); };
  const int cp = (5 - wg % 3) % 3;
  const int ord = cp < 2 ? 0x24 : (ps == 1 ? 0x09 : 0x18);
  auto put_k = [&](int v, const u32x4& z, const u32x4& s) {
    *reinterpret_cast<u32x4*>(kzs + 8 * v) = u32x4{z[0], z[1], s[0], s[1]};
    *reinterpret_cast<u32x4*>(kzs + 8 * v + 4) = u32x4{z[2], z[3], s[2], s[3]};
  };
  auto put_v = [&](int v, const u32x4& z, const u32x4& s) {
#pragma unroll
    for (int h = 0; h < 2; ++h)
      *reinterpret_cast<u32x4*>(vzs + 8 * v + 4 * h) = u32x4{(z[2 * h] & 0xffffu) | (s[2 * h] << 16), (z[2 * h] >> 16) | (s[2 * h] & 0xffff0000u),
                                                             (z[2 * h + 1] & 0xffffu) | (s[2 * h + 1] << 16), (z[2 * h + 1] >> 16) | (s[2 * h + 1] & 0xffff0000u)};
  };
  unsigned char* slot1 = reinterpret_cast<unsigned char*>(kzs) + 3 * 4096;
  const unsigned short* ksg = reinterpret_cast<const unsigned short*>(ks_);
  const unsigned short* kzg = reinterpret_cast<const unsigned short*>(kz_);
  const unsigned short* vsg = reinterpret_cast<const unsigned short*>(vs_);
  const unsigned short* vzg = reinterpret_cast<const unsigned short*>(vz_);

  if (w == NWAVE) {
    unsigned long long* slot = reinterpret_cast<unsigned long long*>(ws + WS_PACE) + (wg / PGS) * (PGS * PLANE);
    const int me = wg % PGS;
    const __amdgpu_buffer_rsrc_t wrs = __builtin_amdgcn_make_buffer_rsrc((void*)ws, 0, WS_BYTES, 0x00020000);
    const unsigned t0 = (unsigned)lds_ld(org + 2);
    const int mem = wg & 1, xh = (min(ps, 2) * NWG + wg) / 3, xo = WS_X + (xh >> 1) * 32768, xb = l0 + ps * NST;
    int xs = 4, par[2] = {0, 0};
    auto line = [=](const int* t, int k) {
      const unsigned r = (t0 >> 12) * 0x9E3779B1u;
      return reinterpret_cast<const unsigned char*>(t) + ((unsigned)xh * (SKV * DH * 4u) + ((5u * k + (r >> 26)) & 63u) * 32768u + (((k + 1) & 2) << 12) + ((k & 2) << 11) + ((k & 4) << 12) + (((r >> 21) + 11u * k) & 31u) * 128u);
    };
    unsigned long long tag = 0;
    {
      int pre = 0;
      if (ps < 3 && mem && lane < 16) pre = *reinterpret_cast<const int*>(line(lane & 8 ? qv : qk, lane & 7));
      __builtin_amdgcn_sched_barrier(0);
      const int l48 = min(lane, 47), nv = (ps < 3 ? MROW : 3 * NROW) * 8 - 1;
      const unsigned gk = sc_off(512 + l48, DH);
      const u32x4 kz = ldnt4(kzg + gk), ks = ldnt4(ksg + gk);
      u32x4 ql[6], vz[5], vs[5];
#pragma unroll
      for (int i = 0; i < 6; ++i)
        ql[i] = ldnt4(reinterpret_cast<const unsigned short*>(q_) + (unsigned)((((i >> 1) * NWG + wg) / 3) * SQ * DH + 8 * (l48 + 48 * (i & 1))));
#pragma unroll
      for (int i = 0; i < 5; ++i) { const unsigned gv = sc_off(min(256 + 64 * i + lane, nv), GRP); vz[i] = ldnt4(vzg + gv); vs[i] = ldnt4(vsg + gv); }
      tag = __hip_atomic_load(reinterpret_cast<unsigned long long*>(ws + WS_CTL + (min(lane, 2) * NWG + wg) / 3 * 128 + 16), __ATOMIC_RELAXED, __HIP_MEMORY_SCOPE_AGENT);
      __builtin_amdgcn_sched_barrier(0);
      asm volatile("" :: "v"(pre));
      put_k(512 + l48, kz, ks);
#pragma unroll
      for (int i = 0; i < 6; ++i) *reinterpret_cast<u32x4*>(qs + 8 * (l48 + 48 * i)) = ql[i];
      asm volatile("s_waitcnt lgkmcnt(0)" ::: "memory");
      if (lane == 0) __hip_atomic_fetch_add(rdy, 1, __ATOMIC_RELAXED, __HIP_MEMORY_SCOPE_WORKGROUP);
      __builtin_amdgcn_sched_barrier(0);
#pragma unroll
      for (int i = 0; i < 5; ++i) put_v(min(256 + 64 * i + lane, nv), vz[i], vs[i]);
      asm volatile("s_waitcnt lgkmcnt(0)" ::: "memory");
      if (ps == 3 && lane == 0) __hip_atomic_fetch_add(rdy, 0x100, __ATOMIC_RELAXED, __HIP_MEMORY_SCOPE_WORKGROUP);
    }
    tag = tag * 0x9E3779B97F4A7C15ull + 0xD1B54A32D192ED03ull;
    const unsigned xlo = __builtin_amdgcn_readlane((unsigned)tag, min(ps, 2)) ^ 0x5bd1e995u, xhi = __builtin_amdgcn_readlane((unsigned)(tag >> 32), min(ps, 2));
    auto xput = [&](int i, int v) { if (lane < 16) __builtin_amdgcn_raw_buffer_store_b64(u32x2{xlo + v, xhi}, wrs, xo + 128 * i + 8 * lane, 0, AUX_SC1); };
    auto xget = [&](int i) {
      asm volatile("" ::: "memory");
      const u32x2 x = __builtin_amdgcn_raw_buffer_load_b64(wrs, xo + 128 * i, 0, AUX_SC1);
      asm volatile("" ::: "memory");
      const unsigned d = __builtin_amdgcn_readfirstlane(x[0]) - xlo;
      return __builtin_amdgcn_readfirstlane(x[1]) == xhi && d <= 4u ? (int)d : 0;
    };
    auto flip = [&]() {
#pragma unroll 1
      for (int v = lane; v < NST * 4; v += 64) {
        u32x4* a = reinterpret_cast<u32x4*>(tab + xb * 32) + v; u32x4* b = reinterpret_cast<u32x4*>(tab + NS * 32) + v;
        const u32x4 x = *a, y = *b;
        *a = y; *b = x;
      }
    };
    if (ps < 3) {
      u32x4 vz[2], vs[2], kz[4], ks[4];
#pragma unroll
      for (int i = 0; i < 2; ++i) { const unsigned gv = sc_off(min(576 + 64 * i + lane, MROW * 8 - 1), GRP); vz[i] = ldnt4(vzg + gv); vs[i] = ldnt4(vsg + gv); }
#pragma unroll
      for (int i = 0; i < 4; ++i) { const unsigned o = sc_off(min(1840 + 64 * i + lane, MROW * 24 - 1), DH); kz[i] = ldnt4(kzg + o); ks[i] = ldnt4(ksg + o); }
      __builtin_amdgcn_sched_barrier(0);
      int x = 0;
      if (!mem) {
#pragma unroll 1
        for (int i = 0; i < 2; ++i) {
          int mn[2] = {1 << 20, 1 << 20};
#pragma unroll 1
          for (int k = -1; k < 2 * NSP; ++k) {
            int d = load_time(line(i ? qv : qk, k & 7));
            if (k < 0 || d < 270) d = 1 << 20;
            if ((k + 1) & 2) mn[1] = min(mn[1], d); else mn[0] = min(mn[0], d);
          }
          x += (FORCE ? (FORCE >> i) & 1 : mn[1] < mn[0]) << i;
        }
        xput(0, 4 - x);
      }
#pragma unroll
      for (int i = 0; i < 2; ++i) put_v(min(576 + 64 * i + lane, MROW * 8 - 1), vz[i], vs[i]);
#pragma unroll
      for (int i = 0; i < 4; ++i) put_k(min(1840 + 64 * i + lane, MROW * 24 - 1), kz[i], ks[i]);
      if (lds_ld(pprog) >= 8 && lane == 0) org[1] = 0;
      if (mem) {
        for (int sp = 0; sp < (1 << 20) && !(x = xget(0)); ++sp) if (lds_ld(pprog) >= 8 && lane == 0) org[1] = 0;
        --x;
      }
      par[0] = x & 1; par[1] = x >> 1;
      for (int sp = 0; sp < (1 << 22) && ((lds_ld(rdy) >> 8) & 255) < NWAVE; ++sp) __builtin_amdgcn_s_sleep(1);
      asm volatile("" ::: "memory");
      if (par[0]) flip();
      asm volatile("s_waitcnt lgkmcnt(0)" ::: "memory");
      if (lane == 0) {
        __hip_atomic_fetch_add(rdy, 0x100, __ATOMIC_RELAXED, __HIP_MEMORY_SCOPE_WORKGROUP);
        if (par[0] == par[1]) __hip_atomic_store(xfl, 1, __ATOMIC_RELAXED, __HIP_MEMORY_SCOPE_WORKGROUP);
      }
      if (par[0] != par[1]) xs = 2;
    }
    auto part = [&](int t) { const int p = (t + 3 - wg % 3) % 3, h = (p * NWG + wg) / 3; return WS_PART + (2 * h + (p == ps ? par[1] : t)) * 4096; };
    int vp0 = part(0) + 16 * lane, vp1 = part(1) + 16 * lane, vf = (lane & 16 ? part(1) : part(0)) + SQ * DH * 4 + 8 * m;
    int lz = (lane & 16) * 256 + SQ * DH * 4 + 8 * (lane & 3);
    const unsigned long long tg = __shfl(tag, (g + 3 - wg % 3) % 3);
    u32x2 tgv = {(unsigned)tg, (unsigned)(tg >> 32)};
    const int ch = (cp * NWG + wg) / 3;
    unsigned ctlo = __builtin_amdgcn_readlane((unsigned)tag, cp), cthi = __builtin_amdgcn_readlane((unsigned)(tag >> 32), cp);
    int so = WS_PART + 2 * ch * 4096;
    int fw = so + (lane & 1) * 4096 + SQ * DH * 4 + 128, eo = WS_CTL + ch * 128 + 16, oo = ch * (SQ * DH * 2) + 8 * lane;
    asm volatile("" : "+v"(vp0), "+v"(vp1), "+v"(vf), "+v"(lz), "+v"(tgv), "+v"(fw), "+v"(eo), "+v"(oo));
    asm volatile("" : "+s"(ctlo), "+s"(cthi), "+s"(so));
    auto xvec = [=](int v, const float* mx) {
      const int ww = v / 86;
      return v < 688 ? reinterpret_cast<unsigned char*>(pl) + ww * (NS * 32) + xb * 32 + 16 * (v - 86 * ww) : (unsigned char*)mx;
    };
    auto xstep = [&](bool tick) {
      if (xs == 2) {
        if (((lds_ld(rdy) >> (ps < 2 ? 24 : 16)) & 255) < NWAVE) return;
        asm volatile("" ::: "memory");
        if (XFL && lane == 0) org[1] = 1;
#pragma unroll 1
        for (int v = lane; v < 704; v += 64) {
          const u32x4 y = *reinterpret_cast<const u32x4*>(xvec(v, stats + (ps * NWAVE + (lane & 7)) * 8));
          __builtin_amdgcn_raw_buffer_store_b128(y, wrs, xo + 1024 + mem * 12288 + 16 * v, 0, AUX_SC1);
        }
        asm volatile("s_waitcnt vmcnt(0)" ::: "memory");
        xput(1 + mem, 1);
        xs = 3;
      } else if (tick && xget(2 - mem)) {
        u32x4 y[11];
#pragma unroll
        for (int i = 0; i < 11; ++i) y[i] = __builtin_amdgcn_raw_buffer_load_b128(wrs, xo + 1024 + (mem ^ 1) * 12288 + 16 * lane + 1024 * i, 0, AUX_SC1);
        flip();
#pragma unroll
        for (int i = 0; i < 11; ++i) *reinterpret_cast<u32x4*>(xvec(lane + 64 * i, xmax + 4 * (lane & 7))) = y[i];
        xs = 4;
        asm volatile("s_waitcnt lgkmcnt(0)" ::: "memory");
        if (lane == 0) __hip_atomic_store(xfl, 3, __ATOMIC_RELAXED, __HIP_MEMORY_SCOPE_WORKGROUP);
        if (XFL && lane == 0) org[1] = 0;
      }
    };
    int last = 0, last8 = 0, nck = CK0, want = 0, own = 0, po = 0, rate = 0, left = 0;
    bool got = true;
    float* park = reinterpret_cast<float*>(kzs);
    int qv3[3];
#pragma unroll
    for (int u = 0; u < 3; ++u) qv3[u] = (lane + 64 * u) / 48;
    constexpr int TOTAL = 2 * NPR;
    int nred = 0;
    for (int spin = 0; nred < 3 && spin < (1 << 23); ++spin) {
      const int n8 = lds_ld(pprog), n = n8 >> 3;
      if (n8 != last8 && n < TOTAL - 4) {
        if (left > 0) {
          const int q = min(n8 - last8, left);
          if (lane == 0) __hip_atomic_fetch_add(org, q * rate, __ATOMIC_RELAXED, __HIP_MEMORY_SCOPE_WORKGROUP);
          left -= q;
          if (left == 0 && lane == 0) org[1] = 0;
        }
        last8 = n8;
      }
      if (last < 1 && n >= 1 && lane == 0) org[1] = 0;
      if (xs < 4) xstep((spin & XTK) == 0);
      if (n != last && n < TOTAL - 4) {
        if (n >= nck) {
          own = lds_ld(org) >> 8;
          if (lane == 0)
            __hip_atomic_store(slot + me * PLANE, ((unsigned long long)nck << 32) | (t0 + (unsigned)own), __ATOMIC_RELAXED, __HIP_MEMORY_SCOPE_AGENT);
          want = nck; nck = nck == CK0 ? CK1 : nck + PCK; left = 0;
          if (lane == 0) org[1] = 0;
          got = lane == me || lane >= PGS; po = 0;
        }
        if (want && n >= want + PDLY) {
          if (!got) {
            const unsigned long long x = __hip_atomic_load(slot + lane * PLANE, __ATOMIC_RELAXED, __HIP_MEMORY_SCOPE_AGENT);
            const int r = (int)((unsigned)x - t0) - own;
            if ((int)(x >> 32) == want && r > -2000 && r < 2000) { got = true; po = r; }
          }
          int e = got ? po : PDLY * 2 * TAU / 10;
#pragma unroll
          for (int k = 1; k < PGS; k <<= 1) e += __shfl_xor(e, k);
          e = __builtin_amdgcn_readfirstlane(e);
          rate = min(max(want == CK0 ? e * 32 / (PGS * SPAN0) : e * 32 / (PGS * SPAN), -RDN * 32), RUP * 32);
          left = want == CK0 ? 8 * SPAN0 : 8 * SPAN; want = 0;
          if (lane == 0) org[1] = rate < -64;
        }
        last = n;
      }
      if ((lds_ld(cnt + nred) | (nred == 2 ? 254 : 0)) == 255) {
        for (int sp = 0; sp < (1 << 22) && ((lds_ld(rdy) >> 16) & 255) < NWAVE; ++sp) __builtin_amdgcn_s_sleep(1);
        asm volatile("" ::: "memory");
        const int pm = (ord >> (2 * nred)) & 3;
        const int t = (pm + wg) % 3;
        const float* st = stats + pm * NWAVE * 8;
        const unsigned char* sl = (nred & 1 ? slot1 : kst) + 16 * lane;
        f32x4 o[3]; float mx[3], sm[3], f[3][NWAVE];
#pragma unroll
        for (int u = 0; u < 3; ++u) {
          mx[u] = -INFINITY;
#pragma unroll
          for (int ww = 0; ww < NWAVE; ++ww) { f[u][ww] = st[ww * 8 + qv3[u]]; mx[u] = fmaxf(mx[u], f[u][ww]); }
#pragma unroll
          for (int ww = 0; ww < NWAVE; ++ww) { f[u][ww] = __expf(f[u][ww] - mx[u]); asm volatile("" : "+v"(f[u][ww])); }
          o[u] = f32x4{0.f, 0.f, 0.f, 0.f}; sm[u] = 0.f;
        }
#pragma unroll
        for (int w2 = 0; w2 < NWAVE; w2 += 2) {
          f32x4 y[2][3]; float z[2][3];
          for (int sp = 0; sp < (1 << 22); ++sp) {
            const int c = __hip_atomic_load(cnt + nred, __ATOMIC_RELAXED, __HIP_MEMORY_SCOPE_WORKGROUP);
            asm volatile("" ::: "memory");
#pragma unroll
            for (int k = 0; k < 2; ++k)
#pragma unroll
              for (int u = 0; u < 3; ++u) {
                y[k][u] = *reinterpret_cast<const f32x4*>(sl + (w2 + k) * OSL + 1024 * u);
                z[k][u] = st[(w2 + k) * 8 + 4 + qv3[u]];
              }
            asm volatile("" ::: "memory");
            if (((__builtin_amdgcn_readfirstlane(c) >> w2) & 3) == 3) break;
          }
#pragma unroll
          for (int k = 0; k < 2; ++k)
#pragma unroll
            for (int u = 0; u < 3; ++u) { o[u] += f[u][w2 + k] * y[k][u]; sm[u] += f[u][w2 + k] * z[k][u]; }
          asm volatile("" : "+v"(o[0]), "+v"(o[1]), "+v"(o[2]), "+v"(sm[0]), "+v"(sm[1]), "+v"(sm[2]));
        }
#pragma unroll
        for (int u = 0; u < 3; ++u) {
          *reinterpret_cast<f32x4*>(park + t * 1024 + 4 * (lane + 64 * u)) = o[u];
          *reinterpret_cast<f32x2*>(park + t * 1024 + SQ * DH + 2 * qv3[u]) = f32x2{mx[u], sm[u]};
        }
        ++nred;
        asm volatile("s_waitcnt lgkmcnt(0)" ::: "memory");
        if (lane == 0) __hip_atomic_store(mrg, nred, __ATOMIC_RELAXED, __HIP_MEMORY_SCOPE_WORKGROUP);
        continue;
      }
      __builtin_amdgcn_s_sleep(4);
    }
    {
      u32x4 y[2][3];
#pragma unroll
      for (int t = 0; t < 2; ++t)
#pragma unroll
        for (int u = 0; u < 3; ++u) y[t][u] = *reinterpret_cast<const u32x4*>(park + t * 1024 + 4 * (lane + 64 * u));
      u32x2 z = *reinterpret_cast<const u32x2*>(reinterpret_cast<const unsigned char*>(park) + lz);
      asm volatile("" : "+v"(z));
#pragma unroll
      for (int t = 0; t < 2; ++t)
#pragma unroll
        for (int u = 0; u < 3; ++u) __builtin_amdgcn_raw_buffer_store_b128(y[t][u], wrs, (t ? vp1 : vp0) + 1024 * u, 0, AUX_SC1);
      if (lane < 32) __builtin_amdgcn_raw_buffer_store_b64(z, wrs, vf, 0, AUX_SC1);
      asm volatile("s_waitcnt vmcnt(0)" ::: "memory");
      if (lane < 32) __builtin_amdgcn_raw_buffer_store_b64(tgv, wrs, vf + 128, 0, AUX_SC1);
    }
    {
      f32x4 x[2][3], ro[3]; u32x2 ms[2][3]; float rm[3], rs[3];
#pragma unroll
      for (int u = 0; u < 3; ++u) {
        ro[u] = *reinterpret_cast<const f32x4*>(park + 2 * 1024 + 4 * (lane + 64 * u));
        const f32x2 a = *reinterpret_cast<const f32x2*>(park + 2 * 1024 + SQ * DH + 2 * qv3[u]);
        rm[u] = a[0]; rs[u] = a[1];
      }
      const float* st = stats + 3 * NWAVE * 8;
      auto try3 = [&](int ww) {
        const int c = __hip_atomic_load(cnt + 3, __ATOMIC_RELAXED, __HIP_MEMORY_SCOPE_WORKGROUP);
        asm volatile("" ::: "memory");
        f32x4 y[3];
#pragma unroll
        for (int u = 0; u < 3; ++u) y[u] = *reinterpret_cast<const f32x4*>(slot1 + ww * OSL + 16 * (lane + 64 * u));
        asm volatile("" ::: "memory");
        const int ok = (__builtin_amdgcn_readfirstlane(c) >> ww) & 1;
        if (ok) {
#pragma unroll
          for (int u = 0; u < 3; ++u) ro[u] += y[u];
        }
        return ok;
      };
      constexpr int TB = 4;
      int nw = 0;
#pragma unroll 1
      for (int spin = 0; spin < (1 << 21); ++spin) {
        asm volatile("" ::: "memory");
        const u32x2 fl = __builtin_amdgcn_raw_buffer_load_b64(wrs, fw, 0, AUX_SC1);
        asm volatile("" ::: "memory");
#pragma unroll 1
        for (int k = 0; k < 4 && nw < NWAVE - TB && try3(nw); ++k) ++nw;
        if (__all(fl[0] == ctlo && fl[1] == cthi)) break;
        __builtin_amdgcn_s_sleep(1);
      }
      asm volatile("" ::: "memory");
#pragma unroll
      for (int t = 0; t < 2; ++t)
#pragma unroll
        for (int u = 0; u < 3; ++u) {
          x[t][u] = __builtin_bit_cast(f32x4, __builtin_amdgcn_raw_buffer_load_b128(wrs, 16 * (lane + 64 * u), so + t * 4096, AUX_SC1));
          ms[t][u] = __builtin_amdgcn_raw_buffer_load_b64(wrs, SQ * DH * 4 + 8 * qv3[u], so + t * 4096, AUX_SC1);
        }
      __builtin_amdgcn_sched_barrier(0);
      for (int q = 0; q < (1 << 22) && ((lds_ld(cnt + 3) >> 8) & 255) != 255; ++q) __builtin_amdgcn_s_sleep(1);
      asm volatile("" ::: "memory");
#pragma unroll
      for (int u = 0; u < 3; ++u)
#pragma unroll
        for (int ww = 0; ww < NWAVE; ++ww) rs[u] += st[ww * 8 + 4 + qv3[u]];
      asm volatile("" : "+v"(rs[0]), "+v"(rs[1]), "+v"(rs[2]));
#pragma unroll 1
      for (int spin = 0; spin < (1 << 22) && nw < NWAVE - TB; ++spin) nw += try3(nw);
      __builtin_amdgcn_sched_barrier(0);
      float fc[3];
#pragma unroll
      for (int u = 0; u < 3; ++u) {
        const float m0 = __uint_as_float(ms[0][u][0]), m1 = __uint_as_float(ms[1][u][0]);
        const float mx = fmaxf(rm[u], fmaxf(m0, m1));
        const float f0 = __expf(m0 - mx), f1 = __expf(m1 - mx), f2 = __expf(rm[u] - mx);
        const float r = 1.f / (f0 * __uint_as_float(ms[0][u][1]) + f1 * __uint_as_float(ms[1][u][1]) + f2 * rs[u]);
        fc[u] = f2 * r;
        x[0][u] = (f0 * r) * x[0][u] + (f1 * r) * x[1][u];
        asm volatile("" : "+v"(x[0][u]), "+v"(fc[u]));
      }
      __builtin_amdgcn_sched_barrier(0);
      const __amdgpu_buffer_rsrc_t ors = __builtin_amdgcn_make_buffer_rsrc((void*)out, 0, NWG * SQ * DH * 2, 0x00020000);
      {
        f32x4 y[TB][3];
        for (int sp = 0; sp < (1 << 22); ++sp) {
          const int c = __hip_atomic_load(cnt + 3, __ATOMIC_RELAXED, __HIP_MEMORY_SCOPE_WORKGROUP);
          asm volatile("" ::: "memory");
#pragma unroll
          for (int u = 0; u < 3; ++u)
#pragma unroll
            for (int k = 0; k < TB; ++k) y[k][u] = *reinterpret_cast<const f32x4*>(slot1 + (NWAVE - TB + k) * OSL + 16 * (lane + 64 * u));
          asm volatile("" ::: "memory");
          if ((__builtin_amdgcn_readfirstlane(c) & 255) == 255) break;
        }
#pragma unroll
        for (int u = 0; u < 3; ++u) {
#pragma unroll
          for (int k = 0; k < TB; ++k) ro[u] += y[k][u];
          const f32x4 o = x[0][u] + fc[u] * ro[u];
          __builtin_amdgcn_raw_buffer_store_b64(u32x2{cvtpk(o[0], o[1]), cvtpk(o[2], o[3])}, ors, oo + 512 * u, 0, AUX_SC1);
        }
        if (lane == 0) __builtin_amdgcn_raw_buffer_store_b64(u32x2{ctlo, cthi}, wrs, eo, 0, AUX_SC1);
      }
    }
    return;
  }

  u32x4 sz[4], ss[4];
  const int vb = (w < 2 ? 256 * w : (w < 6 ? 256 * w + 48 : (w == 7 ? 1584 : 0))) + lane;
  const bool hasc = w < 7 || ps < 3;
  {
    const unsigned short* zg = w != 6 ? kzg : vzg;
    const unsigned short* sg = w != 6 ? ksg : vsg;
    unsigned o[4];
#pragma unroll
    for (int i = 0; i < 4; ++i) o[i] = sc_off(vb + 64 * i, w != 6 ? DH : GRP);
    if (hasc) {
#pragma unroll
      for (int i = 0; i < 4; ++i) { sz[i] = ldnt4(zg + o[i]); ss[i] = ldnt4(sg + o[i]); }
    } else {
#pragma unroll
      for (int i = 0; i < 4; ++i) asm volatile("" : "=v"(sz[i]), "=v"(ss[i]));
    }
    asm volatile("" ::: "memory");
    __builtin_amdgcn_sched_barrier(0);
  }
  const __amdgpu_buffer_rsrc_t krs = __builtin_amdgcn_make_buffer_rsrc((void*)qk, 0, (int)TBYTES, 0x00020000);
  const __amdgpu_buffer_rsrc_t vrs = __builtin_amdgcn_make_buffer_rsrc((void*)qv, 0, (int)TBYTES, 0x00020000);
  const float scale = 0.07216878364870322f;
  unsigned char* myst = kst + w * OSL;
  unsigned short* mypl = pl + w * (NS * 16) + 4 * (m & 3) + g;
  const unsigned short* mytab = tab + w * 4 + g;
  const int mseg = m >> 2;
  unsigned mb = frow(mseg) * GRP;
  unsigned msz = mseg == 3 ? 0 : (mseg == ps ? NROW + XROW : NROW) * GRP;
  auto sbase = [=](int p) { return (unsigned)__builtin_amdgcn_readfirstlane(gbase(p) * (GRP * DH * 4)); };
  const unsigned sb0 = sbase(0), sb1 = sbase(1), sb2 = sbase(2);
  auto soff = [=](int i) { unsigned r = pick((unsigned)(i - l0) < (unsigned)NST, sb0, pick(i < 2 * NST, sb1, sb2)); asm volatile("" : "+s"(r)); return r; };
  auto voff = [&](int e) { int v = (e & 0x7fff) * (DH * 4) + 16 * m; asm volatile("" : "+v"(v)); return v; };
  auto issue = [&](const __amdgpu_buffer_rsrc_t& rs, i32x4 (&x)[3], int vo, unsigned so) {
#pragma unroll
    for (int u = 0; u < 3; ++u) x[u] = ldbuf(rs, vo + 256 * u, so);
  };

  constexpr int T256 = TAU * 256 / 10;
  unsigned tbase;
  auto now = [&]() { return (int)((unsigned)__builtin_amdgcn_s_memrealtime() - tbase); };
  auto grid = [&](int k) { return ((8 * k + w) * (T256 / 8)) >> 8; };
  auto wait_t = [&](int rel) {
    __builtin_amdgcn_sched_barrier(0);
    for (;;) {
      const int d = rel - now();
      if (d <= 0) break;
      if (d > 12) __builtin_amdgcn_s_sleep(2);
    }
    __builtin_amdgcn_sched_barrier(0);
  };
  int go = 0, grel = 0;
  auto gate = [&](int k) {
    const int rel = grid(k);
    __builtin_amdgcn_sched_barrier(0);
    const unsigned long long of = __hip_atomic_load(reinterpret_cast<unsigned long long*>(org), __ATOMIC_RELAXED, __HIP_MEMORY_SCOPE_WORKGROUP);
    int t = now();
    int o = __builtin_amdgcn_readfirstlane((int)of) >> 8;
    const int fl = __builtin_amdgcn_readfirstlane((int)(of >> 32));
    while (o + rel - t > 18) { __builtin_amdgcn_s_sleep(2); o = lds_ld(org) >> 8; t = now(); }
    go = o; grel = rel + GLATE;
    __builtin_amdgcn_sched_barrier(0);
    if (fl) wait_tv<3>(tbase + (unsigned)(o + rel), o + rel - t);
    else wait_tv<0>(tbase + (unsigned)(o + rel), o + rel - t);
    __builtin_amdgcn_sched_barrier(0);
  };
  auto stamp = [&]() {
    __builtin_amdgcn_sched_barrier(0);
    const int o = now() - grel;
    if (o > go) lds_max(org, o << 8);
    __builtin_amdgcn_sched_barrier(0);
  };

  i32x4 xa[3], xb[3], xc[3], xd[3];
  const int s0 = ord == 0x09 ? 2 * NST - RS : NST - (cp == 0 ? RS : 0), s1 = cp == 2 ? NS - 2 * RS : 2 * NST - RS;
  auto vidx_f = [=](int j) {
    int s = j;
    if (j >= NST - RS && j < 2 * NST - RS) s += ps == 1 ? RS : (cp == 0 && j < NST ? NST : 0);
    if (j >= 2 * NST - RS && j < 2 * NST) s += NST;
    if (cp == 2) { if (j >= NS - 2 * RS) s += j < NS - RS ? RS - (ps == 1 ? 2 : 1) * NST : -RS; }
    else if (j >= NS - RS) s += (cp - 2) * NST;
    return s < NST ? s + l0 : (s < NST + l0 ? s - NST : s);
  };
  int vperm = vidx_f(lane) | (vidx_f(64 + lane) << 8) | ((vidx_f(128 + lane) & 255) << 16);
  asm volatile("" : "+v"(vperm));
  const unsigned tb0 = (unsigned)lds_ld(org + 2);
#pragma unroll
  for (int i = 0; i < 3; ++i) {
    const int s = lane + 64 * i, p = s >= 2 * NST ? 2 : ((unsigned)(s - l0) < (unsigned)NST ? 0 : 1);
    int n = NWAVE * (s % NST) + w + SLIP * (wg % SLM);
    if (n >= NQ) n -= NQ;
    const int b = n / NST, add = lrow(p) * GRP;
    u32x2 e;
#pragma unroll
    for (int h = 0; h < 2; ++h) {
      const int hh = 16 * ((p + wg) % 3) + 2 * b + h, k = (256 * hh + 2) / 3 + 2 * (n - NST * b);
      e[h] = (unsigned)(k + add) | ((unsigned)(k + 1 < (256 * hh + 258) / 3 ? k + 1 + add : k + add + 0x8000) << 16);
    }
    if (s < NS && p != ps) *reinterpret_cast<u32x2*>(tab + s * 32 + w * 4) = e;
  }
  if (ps < 3 && w >= 4) {
    unsigned short* tb = tab + (w & 2 ? NS : l0 + ps * NST) * 32 + 2 * (w & 1);
    const int sg = 8 * (lane >> 1) + 4 * (w & 1) + 2 * (lane & 1) + ((w >> 1) & 1), add = lrow(ps) * GRP;
    const int k0 = (32 * sg + 1) / 3 + add, k1 = (32 * sg + 33) / 3 + add;
    const int i0 = 11 * lane - __popcll(__ballot(k1 - k0 == 10) & ((1ull << lane) - 1));
    int q = i0 + i0 / 137, r = i0 % 137;
    for (int k = k0; k < k1; ++k, ++q) {
      tb[(q >> 1) * 4 + (q & 1)] = (unsigned short)k;
      if (++r == 137) { tb[(q >> 1) * 4 + 1] = (unsigned short)(k + 0x8000); ++q; }
    }
    if (lane == 63) for (; q < 2 * NQ; ++q) tb[(q >> 1) * 4 + (q & 1)] = (unsigned short)(k1 - 1 + 0x8000);
  }
  asm volatile("" ::: "memory");
  int E0 = mytab[0], E1 = mytab[32], E2 = mytab[64];
  const int v0 = voff(E0), v1 = voff(E1);
  asm volatile("" : "+v"(E0), "+v"(E1), "+v"(E2));
  int Ea = 0, Eb = 0, Ec = 0, vo1 = 0;
  auto prologue = [&]() {
    tbase = tb0;
    Ea = E0; Eb = E1; Ec = E2; vo1 = v1;
    __builtin_amdgcn_sched_barrier(0);
    auto puts = [&]() {
      if (w != 6) {
#pragma unroll
        for (int i = 0; i < 4; ++i) put_k(vb + 64 * i, sz[i], ss[i]);
      } else {
#pragma unroll
        for (int i = 0; i < 4; ++i) put_v(vb + 64 * i, sz[i], ss[i]);
      }
      asm volatile("s_waitcnt lgkmcnt(0)" ::: "memory");
      if (lane == 0) __hip_atomic_fetch_add(rdy, w < 2 ? 0x101 : 0x100, __ATOMIC_RELAXED, __HIP_MEMORY_SCOPE_WORKGROUP);
    };
    if (w < 2) puts();
    wait_t(ORG0 + grid(0));
    issue(krs, xa, v0, soff(0));
    __builtin_amdgcn_sched_barrier(0);
    if (w >= 2 && hasc) puts();
  };
  int woff[3];
#pragma unroll
  for (int u = 0; u < 3; ++u) {
    const int qq = 8 * u + (m >> 1);
    woff[u] = qq * 128 + (((2 * g) ^ qq) & 7) * 16 + (m & 1) * 8;
  }
  const int roff = g * 128 + ((2 * (m >> 2) + (m & 1)) ^ g) * 16;
  auto vidx = [&](int j) { return (__builtin_amdgcn_readlane(vperm, j & 63) >> (8 * (j >> 6))) & 255; };

  bf16x8 qf[6];
  u32x4 zs0[3];
  float mrun = -INFINITY;
  int Ep0 = 0x8000, Ep1 = 0x8000;
  auto kzread = [&](u32x4 (&zs)[3], int e) {
    const unsigned* zp = kzs + ((e >> 6) & 0x1ff) * DH + 4 * m;
#pragma unroll
    for (int u = 0; u < 3; ++u) zs[u] = *reinterpret_cast<const u32x4*>(zp + 64 * u);
  };
  auto kdeq = [&](const i32x4 (&x)[3], const u32x4 (&zs)[3], int c) {
#pragma unroll
    for (int u = 0; u < 3; ++u) {
      const i32x4 y = x[u];
      u32x2 o = {deq2(y[0], y[1], zs[u][0], zs[u][2]), deq2(y[2], y[3], zs[u][1], zs[u][3])};
      *reinterpret_cast<u32x2*>(myst + (woff[u] ^ (c << 4))) = o;
    }
  };
  auto kread = [&](u32x4 (&a)[6]) {
#pragma unroll
    for (int j = 0; j < 6; ++j) a[j] = *reinterpret_cast<const u32x4*>(myst + ((roff + 512 * j) ^ ((j & 1) << 6)));
  };
  auto kfin = [&](const u32x4 (&a)[6], int e0, int e1, int i) {
    f32x4 acc = {0.f, 0.f, 0.f, 0.f};
#pragma unroll
    for (int j = 0; j < 6; ++j) acc = __builtin_amdgcn_mfma_f32_16x16x32_bf16(__builtin_bit_cast(bf16x8, a[j]), qf[j], acc, 0, 0, 0);
    const unsigned o = cvtpk(bfround(acc[0]) * scale, bfround(acc[1]) * scale);
    if ((unsigned)e0 - mb < msz) { mrun = fmaxf(mrun, bflo(o)); mypl[i * 16] = (unsigned short)o; }
    if ((unsigned)e1 - mb < msz) { mrun = fmaxf(mrun, bfhi(o)); mypl[i * 16 + 16] = (unsigned short)(o >> 16); }
  };
  auto kissue = [&](i32x4 (&x)[3], int vo, unsigned so) { issue(krs, x, vo, so); stamp(); };
#pragma unroll
  for (int j = 0; j < 6; ++j) asm volatile("" : "=v"(qf[j]));
#pragma unroll
  for (int u = 0; u < 3; ++u) asm volatile("" : "=v"(zs0[u]));
  auto adopt = [&]() {
    int x = 0;
    for (int spin = 0; spin < (1 << 22) && !(x = lds_ld(xfl)); ++spin) __builtin_amdgcn_s_sleep(1);
    asm volatile("" ::: "memory");
    if ((x & 2) && mseg == ps) { mrun = xmax[4 * w + (m & 3)]; stats[(ps * NWAVE + w) * 8 + (m & 3)] = mrun; }
  };
  static_assert(LD % 2 == 0 && LD > 4 && LD <= NST - RS, "");
  const unsigned long long spec = 1ull | (1ull << (ps ? 8 : LD / 2 - 2)) | (ps < 2 ? (1ull << 44) | (1ull << 60) : 0ull);
  prologue();
  {
#pragma unroll 1
    for (int i = 0; i < NS - 1; i += 2) {
      const unsigned so1 = soff(i + 1), so2 = soff(i + 2);
      gate(i + 1);
      kissue(xb, vo1, so1);
      if ((spec >> (i >> 1)) & 1) {
        if (i < 88) {
          const int sh = i ? 8 : 0, need = i ? (ps < 3 ? NWAVE + 1 : NWAVE) : 3;
          for (int spin = 0; spin < (1 << 22) && ((lds_ld(rdy) >> sh) & 255) < need; ++spin) __builtin_amdgcn_s_sleep(1);
          asm volatile("" ::: "memory");
          if (i == 0) {
            kzread(zs0, Ea);
#pragma unroll
            for (int j = 0; j < 6; ++j)
              qf[j] = *reinterpret_cast<const bf16x8*>(qs + min(m, 3 * SQ - 1) * DH + 32 * j + 8 * g);
          }
        } else if (i == 88) {
          mrun = rows<true>(mrun);
          if (lane < 2 * SQ) stats[(mseg * NWAVE + w) * 8 + (m & 3)] = mrun;
          lds_add(rdy, 1 << 24);
        } else adopt();
      }
      u32x4 a[6], zs1[3];
      kread(a);
      kzread(zs1, Eb);
      const int in = i + 3 < NS ? i + 3 : l0;
      const int En0 = mytab[in * 32], En1 = mytab[in * 32 + 32];
      kdeq(xa, zs0, 0);
      kfin(a, Ep0, Ep1, i - 2);
      const int vo2 = voff(Ec);
      gate(i + 2);
      kissue(xa, vo2, so2);
      kzread(zs0, Ec);
      kdeq(xb, zs1, 1);
      lds_add(pprog, 1);
      Ep0 = Ea; Ep1 = Eb; Ea = Ec; Eb = En0; Ec = En1;
      vo1 = voff(Eb);
    }
  }
  f32x4 oacc[3][4];
  float srun;
  unsigned z0;
  auto vdeq = [&](const i32x4& y, unsigned zs, float (&f)[4]) {
    const float z = bflo(zs), s = bfhi(zs);
    const unsigned t0 = cvtpk((float)y[0] - z, (float)y[1] - z), t1 = cvtpk((float)y[2] - z, (float)y[3] - z);
    f[0] = bflo(t0) * s; f[1] = bfhi(t0) * s; f[2] = bflo(t1) * s; f[3] = bfhi(t1) * s;
  };
  auto prob = [&](int e, unsigned lg) { return __expf(__uint_as_float((unsigned)e - mb < msz ? lg << 16 : 0xff800000u) - mrun); };
  {
    gate(NS);
    issue(vrs, xb, vo1, sb0);
    stamp();
    u32x4 a[6];
    kread(a);
    const int En0 = mytab[l0 * 32 + 64], En1 = mytab[l0 * 32 + 96];
    const unsigned zv = vzs[Eb & 0x7fff], lg = mypl[l0 * 16];
    z0 = vzs[Ec & 0x7fff];
    kdeq(xa, zs0, 0);
    kfin(a, Ep0, Ep1, NS - 3);
    const int vo2 = voff(Ec);
    gate(NS + 1);
    issue(vrs, xa, vo2, sb0);
    stamp();
    kread(a);
    unsigned bfr[3][4];
#pragma unroll
    for (int db = 0; db < 3; ++db) {
      float f[4];
      vdeq(xb[db], zv, f);
#pragma unroll
      for (int c = 0; c < 4; ++c) bfr[db][c] = cvtpk(f[c], 0.f);
      if (db == 0) { asm volatile("" : "+v"(bfr[0][0]), "+v"(bfr[0][1]), "+v"(bfr[0][2]), "+v"(bfr[0][3])); }
    }
    kfin(a, Ea, 0x8000, NS - 1);
    mrun = rows<true>(mrun);
    if (lane < 3 * SQ) stats[(mseg * NWAVE + w) * 8 + (m & 3)] = mrun;
    lds_add(rdy, 0x10000);
    if (mseg == 3) mrun = 0.f;
    srun = prob(Eb, lg);
    const u32x2 pa = {cvtpk(srun, 0.f), 0u};
#pragma unroll
    for (int db = 0; db < 3; ++db)
#pragma unroll
      for (int c = 0; c < 4; ++c)
        oacc[db][c] = __builtin_amdgcn_mfma_f32_16x16x16bf16_1k(__builtin_bit_cast(bf16x4, pa), __builtin_bit_cast(bf16x4, u32x2{bfr[db][c], 0u}), f32x4{0.f, 0.f, 0.f, 0.f}, 0, 0, 0);
    lds_add(pprog, 2);
    Ea = Ec; Eb = En0; Ec = En1;
    vo1 = voff(Eb);
  }

  auto vend = [&](int p, int k) {
    const int pm = (ord >> (2 * p)) & 3;
    float* oslot = reinterpret_cast<float*>(p & 1 ? slot1 + w * OSL : myst);
    if (g == pm) {
#pragma unroll
      for (int db = 0; db < 3; ++db)
        if (k >> db & 1) {
#pragma unroll
          for (int r = 0; r < 4; ++r)
            *reinterpret_cast<f32x4*>(oslot + r * DH + 64 * db + 4 * m) = f32x4{oacc[db][0][r], oacc[db][1][r], oacc[db][2][r], oacc[db][3][r]};
          asm volatile("" ::: "memory");
        }
    }
    if (k & 8) {
      const float sr = rows<false>(srun);
      if (g == pm && mseg == pm) stats[(pm * NWAVE + w) * 8 + 4 + (m & 3)] = sr;
      lds_add(cnt + p, 1 << w);
    }
  };
  int El = 0, Ia = l0 + 1, Ib = l0 + 2, Ic = l0 + 3;
  unsigned so1 = sb0;
  auto vpair = [&](auto last, int j) {
    constexpr bool LAST = decltype(last)::value;
    gate(NS + j + 1);
    issue(vrs, xb, vo1, so1);
    stamp();
    const unsigned z1 = vzs[Eb & 0x7fff], lg0 = mypl[Ia * 16], lg1 = mypl[Ib * 16];
    const unsigned so2 = soff(Ic);
    Ia = Ic; Ib = vidx(j + 3); Ic = vidx(j + 4); so1 = soff(Ib);
    const int En0 = mytab[Ib * 32], En1 = mytab[Ic * 32];
    if constexpr (LAST) El = mytab[vidx(j + 5) * 32];
    float f[3][4];
#pragma unroll
    for (int db = 0; db < 3; ++db) {
      vdeq(xa[db], z0, f[db]);
      asm volatile("" : "+v"(f[db][0]), "+v"(f[db][1]), "+v"(f[db][2]), "+v"(f[db][3]));
    }
    const float p0 = prob(Ea, lg0), p1 = prob(Eb, lg1);
    srun += p0 + p1;
    u32x2 pa = {cvtpk(p0, p1), 0u};
    asm volatile("" : "+v"(pa[0]), "+v"(srun));
    const int vo2 = voff(Ec);
    int vn[3];
    if constexpr (LAST) { vn[0] = voff(En0); vn[1] = voff(En1); vn[2] = voff(El); }
    gate(NS + j + 2);
    issue(vrs, xa, vo2, so2);
    if constexpr (LAST) __builtin_amdgcn_sched_barrier(0); else stamp();
    z0 = vzs[Ec & 0x7fff];
    unsigned bfr[3][4];
#pragma unroll
    for (int db = 0; db < 3; ++db) {
      float y[4];
      vdeq(xb[db], z1, y);
#pragma unroll
      for (int c = 0; c < 4; ++c) bfr[db][c] = cvtpk(f[db][c], y[c]);
      if (!LAST && db == 0) { asm volatile("" : "+v"(bfr[0][0]), "+v"(bfr[0][1]), "+v"(bfr[0][2]), "+v"(bfr[0][3])); }
    }
#pragma unroll
    for (int db = 0; db < 3; ++db)
#pragma unroll
      for (int c = 0; c < 4; ++c)
        oacc[db][c] = __builtin_amdgcn_mfma_f32_16x16x16bf16_1k(__builtin_bit_cast(bf16x4, pa), __builtin_bit_cast(bf16x4, u32x2{bfr[db][c], 0u}), oacc[db][c], 0, 0, 0);
    Ea = Ec; Eb = En0; Ec = En1;
    if constexpr (!LAST) vo1 = voff(Eb);
    else {
      const int tr = go + grel - GLATE;
      wait_t(tr + SD);
      issue(vrs, xc, vn[0], so2);
      wait_t(tr + 2 * SD);
      issue(vrs, xd, vn[1], so2);
      wait_t(tr + 3 * SD);
      issue(vrs, xb, vn[2], so2);
      __builtin_amdgcn_sched_barrier(0);
    }
  };
#pragma unroll 1
  for (int j = 1; j < NS - 6; j += 2) {
    vpair(IC<0>{}, j);
    lds_add(pprog, 1);
    const unsigned d0 = j + 2 - (s0 | 1), d1 = j + 2 - (s1 | 1);
    if (d0 <= 4u || d1 <= 4u) vend(d0 > 4u, ((d0 > 4u && cp == 2 ? 0x0c3 : 0xc21) >> (2 * min(d0, d1))) & 15);
    if (j == (cp == 2 ? 99 : 71)) {
      for (int spin = 0; spin < (1 << 22) && lds_ld(mrg) < 1; ++spin) __builtin_amdgcn_s_sleep(1);
      asm volatile("" ::: "memory");
      if (ps == 2) adopt();
      if (mseg == 3) {
        mrun = -INFINITY;
#pragma unroll
        for (int ww = 0; ww < NWAVE; ++ww) mrun = fmaxf(mrun, stats[(cp * NWAVE + ww) * 8 + (m & 3)]);
      }
    }
  }
  static_assert(RS == 6, "");
  vend(2, 15);
  mb = frow(cp) * GRP; msz = mseg == 3 ? NROW * GRP : 0;
  vpair(IC<1>{}, NS - 6);
  float* oslot = reinterpret_cast<float*>(slot1 + w * OSL);
  u32x2 pa;
  unsigned z1, z2, z3;
  {
    const int I = Ia;
    z1 = vzs[Eb & 0x7fff]; z2 = vzs[Ec & 0x7fff]; z3 = vzs[El & 0x7fff];
    const unsigned lg0 = mypl[I * 16], lg1 = mypl[I * 16 + 16], lg2 = mypl[I * 16 + 32], lg3 = mypl[I * 16 + 48];
    const float p0 = prob(Ea, lg0), p1 = prob(Eb, lg1), p2 = prob(Ec, lg2), p3 = prob(El, lg3);
    srun += p0 + p1;
    srun += p2 + p3;
    pa = u32x2{cvtpk(p0, p1), cvtpk(p2, p3)};
    const float sr = rows<false>(srun);
    if (lane >= 60) stats[(3 * NWAVE + w) * 8 + 4 + (m & 3)] = sr;
    lds_add(cnt + 3, 0x100 << w);
    for (int spin = 0; spin < (1 << 22) && lds_ld(mrg) < 2; ++spin) __builtin_amdgcn_s_sleep(1);
    asm volatile("" : "+v"(pa), "+v"(z1), "+v"(z2), "+v"(z3) :: "memory");
  }
  __builtin_amdgcn_sched_barrier(0);
  unsigned b01[3][4];
  float fd[3][4];
#pragma unroll
  for (int db = 0; db < 3; ++db) vdeq(xa[db], z0, fd[db]);
#pragma unroll
  for (int db = 0; db < 3; ++db) {
    float y[4];
    vdeq(xc[db], z1, y);
#pragma unroll
    for (int c = 0; c < 4; ++c) b01[db][c] = cvtpk(fd[db][c], y[c]);
  }
#pragma unroll
  for (int db = 0; db < 3; ++db) {
    vdeq(xd[db], z2, fd[db]);
    asm volatile("" : "+v"(fd[db][0]), "+v"(fd[db][1]), "+v"(fd[db][2]), "+v"(fd[db][3]), "+v"(b01[db][0]), "+v"(b01[db][1]), "+v"(b01[db][2]), "+v"(b01[db][3]));
  }
  __builtin_amdgcn_sched_barrier(0);
#pragma unroll
  for (int db = 0; db < 3; ++db) {
    float y[4];
    vdeq(xb[db], z3, y);
#pragma unroll
    for (int c = 0; c < 4; ++c)
      oacc[db][c] = __builtin_amdgcn_mfma_f32_16x16x16bf16_1k(__builtin_bit_cast(bf16x4, pa), __builtin_bit_cast(bf16x4, u32x2{b01[db][c], cvtpk(fd[db][c], y[c])}), oacc[db][c], 0, 0, 0);
    if (g == 3) {
#pragma unroll
      for (int r = 0; r < 4; ++r)
        *reinterpret_cast<f32x4*>(oslot + r * DH + 64 * db + 4 * m) = f32x4{oacc[db][0][r], oacc[db][1][r], oacc[db][2][r], oacc[db][3][r]};
    }
    __builtin_amdgcn_sched_barrier(0);
  }
  lds_add(cnt + 3, 1 << w);
}

}

extern "C" void kernel_launch(void* const* d_in, const int* in_sizes, int n_in,
                              void* d_out, int out_size, void* d_ws, size_t ws_size,
                              hipStream_t stream) {
  const __hip_bfloat16* q  = (const __hip_bfloat16*)d_in[0];
  const int* qk            = (const int*)d_in[1];
  const int* qv            = (const int*)d_in[2];
  const __hip_bfloat16* ks = (const __hip_bfloat16*)d_in[3];
  const __hip_bfloat16* kz = (const __hip_bfloat16*)d_in[4];
  const __hip_bfloat16* vs = (const __hip_bfloat16*)d_in[5];
  const __hip_bfloat16* vz = (const __hip_bfloat16*)d_in[6];
  kivi_attn_kernel<<<NWG, NTHR + 64, 0, stream>>>(q, qk, qv, ks, kz, vs, vz, (__hip_bfloat16*)d_out, (unsigned char*)d_ws);
}
```

```cpp
#include <hip/hip_runtime.h>
#include <hip/hip_bf16.h>
#include <stdint.h>

namespace {

constexpr int B_ = 8, H_ = 32, SQ = 4, SKV = 4096, DH = 192, GRP = 64, NGK = SKV / GRP;
constexpr int NWG = B_ * H_;
constexpr int NWAVE = 8, NTHR = NWAVE * 64;
constexpr int NST = 43;
constexpr int NQ = NWAVE * NST;
constexpr int NS = 3 * NST;
constexpr int NPR = (NS + 1) / 2;
constexpr int NROW = 22;
constexpr unsigned TBYTES = (unsigned)NWG * SKV * DH * 4u;
constexpr int RS = 6;
#define SLIP 2
#define SLM 5
#define TAU 785
#define ORG0 285
#define GLATE 17
#define SHM 18
#define LD 14
#define NSP 3
#define XL 1
#define XAUX 18
#define XAB 0
#define PAT 1
#define XLA 18
#define HLA 16
#define FLA 16
#define PLA 16
#define HL 1
#define XQ 0
#define HSA 16
#define OSA 16
#define PTH 0
#define CW 1
#define ALLFL 0
#define PT 1
#define XFL 1
#define FORCE 0
constexpr int XROW = NROW - 1;
constexpr int MROW = 3 * NROW + XROW;
constexpr int SD = 20;
constexpr int PGS = 8, PLANE = 544;
constexpr int CK0 = 12, CK1 = 46, PCK = 31, PDLY = 3;
constexpr int SPAN0 = 30, SPAN = 16, RDN = 8, RUP = 20;
constexpr int WS_CTL = 1 << 18;
constexpr int WS_PART = 1 << 19;
constexpr int WS_PACE = 4 << 20;
constexpr int WS_X = 12 << 20;
constexpr int WS_Y = 16 << 20;
constexpr int WS_BYTES = WS_Y + (NWG / 8) * 131072;
constexpr int AUX_NT = 2, AUX_SC1 = 16;
constexpr int OSL = SQ * DH * 4;

using u32x4  = __attribute__((ext_vector_type(4))) unsigned;
using u32x2  = __attribute__((ext_vector_type(2))) unsigned;
using i32x4  = __attribute__((ext_vector_type(4))) int;
using f32x4  = __attribute__((ext_vector_type(4))) float;
using bf16x8 = __attribute__((ext_vector_type(8))) short;
using bf16x4 = __attribute__((ext_vector_type(4))) short;

using f32x2  = __attribute__((ext_vector_type(2))) float;
using bfx2   = __attribute__((ext_vector_type(2))) __bf16;
__device__ __forceinline__ unsigned cvtpk(float lo, float hi) {
  f32x2 f = {lo, hi};
  return __builtin_bit_cast(unsigned, __builtin_convertvector(f, bfx2));
}
__device__ __forceinline__ float bflo(unsigned p) { return __uint_as_float(p << 16); }
__device__ __forceinline__ float bfhi(unsigned p) { return __uint_as_float(p & 0xffff0000u); }
__device__ __forceinline__ float bfround(float x) { return bflo(cvtpk(x, x)); }

__device__ __forceinline__ u32x4 ldnt4(const unsigned short* p) { return __builtin_nontemporal_load(reinterpret_cast<const u32x4*>(p)); }

__device__ __forceinline__ i32x4 ldbuf(__amdgpu_buffer_rsrc_t r, int voff, unsigned soff) {
  return __builtin_bit_cast(i32x4, __builtin_amdgcn_raw_buffer_load_b128(r, voff, (int)soff, AUX_NT));
}

__device__ __forceinline__ unsigned deq2(int c0, int c1, unsigned z, unsigned s) {
  unsigned t = cvtpk((float)c0 - bflo(z), (float)c1 - bfhi(z));
  return cvtpk(bflo(t) * bflo(s), bfhi(t) * bfhi(s));
}

__device__ __forceinline__ unsigned lds_addr(int* p) { return (unsigned)(size_t)(__attribute__((address_space(3))) int*)p; }
__device__ __forceinline__ void lds_add(int* p, int v) {
  asm volatile("s_mov_b64 exec, 1\n\tds_add_u32 %0, %1\n\ts_mov_b64 exec, -1" :: "v"(lds_addr(p)), "v"(v) : "memory");
}
__device__ __forceinline__ void lds_max(int* p, int v) {
  asm volatile("s_mov_b64 exec, 1\n\tds_max_i32 %0, %1\n\ts_mov_b64 exec, -1" :: "v"(lds_addr(p)), "v"(v) : "memory");
}

template <bool MAX> __device__ __forceinline__ float rows(float x) {
  const auto a = __builtin_amdgcn_permlane16_swap(__float_as_uint(x), __float_as_uint(x), false, false);
  x = MAX ? fmaxf(__uint_as_float(a[0]), __uint_as_float(a[1])) : __uint_as_float(a[0]) + __uint_as_float(a[1]);
  const auto b = __builtin_amdgcn_permlane32_swap(__float_as_uint(x), __float_as_uint(x), false, false);
  return MAX ? fmaxf(__uint_as_float(b[0]), __uint_as_float(b[1])) : __uint_as_float(b[0]) + __uint_as_float(b[1]);
}

template <int N> __device__ __forceinline__ void wait_tv(unsigned tg, int d) {
  asm volatile("s_cmp_lt_i32 %0, 1\n\ts_cbranch_scc1 2f\n1:\n\ts_memrealtime vcc\n\ts_waitcnt lgkmcnt(0)\n\ts_sub_i32 %0, %1, vcc_lo\n\t"
               "s_cmp_gt_i32 %0, 0\n\ts_cbranch_scc1 1b\n2:\n\ts_waitcnt vmcnt(%2)" : "+s"(d) : "s"(tg), "n"(N) : "memory", "scc", "vcc");
}

__device__ __forceinline__ void lds_barrier() { asm volatile("s_waitcnt lgkmcnt(0)\n\ts_barrier" ::: "memory"); }

__device__ __forceinline__ int lds_ld(const int* p) {
  return __builtin_amdgcn_readfirstlane(__hip_atomic_load(p, __ATOMIC_RELAXED, __HIP_MEMORY_SCOPE_WORKGROUP));
}

__device__ __forceinline__ int load_time(const void* q) {
  unsigned long long t0, t1; int x;
  const unsigned long long p = (unsigned)__builtin_amdgcn_readfirstlane((unsigned)(size_t)q) | ((unsigned long long)(unsigned)__builtin_amdgcn_readfirstlane((unsigned)((size_t)q >> 32)) << 32);
  asm volatile("s_memtime %0\n\ts_waitcnt lgkmcnt(0)\n\ts_load_dword %2, %3, 0x0\n\ts_waitcnt lgkmcnt(0)\n\ts_memtime %1\n\ts_waitcnt lgkmcnt(0)"
               : "=&s"(t0), "=&s"(t1), "=&s"(x) : "s"(p) : "memory");
  return (int)(t1 - t0);
}

__device__ __forceinline__ unsigned pick(bool c, unsigned a, unsigned b) { return c ? a : b; }

template <int N> struct IC { static constexpr int value = N; };

__global__ __launch_bounds__(NTHR + 64, 3)
void kivi_attn_kernel(const __hip_bfloat16* __restrict__ q_, const int* __restrict__ qk, const int* __restrict__ qv,
                      const __hip_bfloat16* __restrict__ ks_, const __hip_bfloat16* __restrict__ kz_,
                      const __hip_bfloat16* __restrict__ vs_, const __hip_bfloat16* __restrict__ vz_,
                      __hip_bfloat16* __restrict__ out, unsigned char* ws) {
  __shared__ __attribute__((aligned(16))) unsigned char kst[NWAVE * OSL];
  __shared__ __attribute__((aligned(16))) unsigned short pl[NWAVE * NS * 16];
  __shared__ __attribute__((aligned(16))) unsigned kzs[MROW * DH];
  __shared__ __attribute__((aligned(16))) unsigned vzs[MROW * GRP];
  __shared__ __attribute__((aligned(16))) unsigned short tab[(NS + NST) * NWAVE * 4];
  __shared__ __attribute__((aligned(16))) unsigned short qs[3 * SQ * DH];
  __shared__ __attribute__((aligned(16))) float stats[4 * NWAVE * 8];
  __shared__ __attribute__((aligned(16))) float xmax[NWAVE * 4];
  __shared__ int cnt[4], rdy[1], pprog[1], mrg[1], xfl[1];
  __shared__ __attribute__((aligned(8))) int org[4];

  const int wg = (blockIdx.x & ~7) | ((blockIdx.x & 3) << 1) | ((blockIdx.x >> 2) & 1);
  const int tid = threadIdx.x, lane = tid & 63;
  const unsigned char* ca; int cn;
  asm volatile("s_getpc_b64 %0\n.Lkivi_code_a:\n\ts_mov_b32 %1, .Lkivi_code_b-.Lkivi_code_a" : "=s"(ca), "=s"(cn));
  const unsigned cw = __builtin_amdgcn_raw_buffer_load_b32(__builtin_amdgcn_make_buffer_rsrc((void*)ca, 0, cn, 0x00020000), tid * (CW == 2 ? 64 : 128), 0, 0);
  __builtin_amdgcn_sched_barrier(0);
  const int w = __builtin_amdgcn_readfirstlane(tid >> 6);
  const int m = lane & 15, g = lane >> 4;
  if (tid == 0) {
    rdy[0] = 0; pprog[0] = 0; cnt[0] = 0; cnt[1] = 0; cnt[2] = 0; cnt[3] = 0; mrg[0] = 0; xfl[0] = 0;
    org[0] = ORG0 << 8; org[1] = 1; org[2] = (int)(unsigned)__builtin_amdgcn_s_memrealtime();
  }
  lds_barrier();

  const int ps = wg % SHM / 2 == 3 ? 0 : ((wg + NWG) % SHM / 2 == 3 ? 1 : ((wg + 2 * NWG) % SHM / 2 == 3 ? 2 : 3));
  const int l0 = ps == 0 ? LD : 0;
  const int r2 = 2 * NROW + (ps < 2 ? XROW : 0);
  auto frow = [=](int p) { return p == 2 ? r2 : ((p == 0) == (ps == 0) ? NROW : 0); };
  auto lrow = [=](int p) { return frow(p) - (p == ps ? 0 : (p + wg) % 3 * (NROW - 1)); };
  auto gbase = [=](int p) { return (p * NWG + wg) / 3 * NGK - lrow(p); };
  const int gba = gbase(ps == 0), gbb = gbase(ps != 0), gb2 = gbase(2);
  auto sc_off = [=](int v, int RL) { return (unsigned)(8 * v + RL * (gba + (8 * v >= NROW * RL ? gbb - gba : 0) + (8 * v >= r2 * RL ? gb2 - gbb : 0))); };
  const int cp = (5 - wg % 3) % 3;
  const int ord = cp < 2 ? 0x24 : (ps == 1 ? 0x09 : 0x18);
  auto put_k = [&](int v, const u32x4& z, const u32x4& s) {
    *reinterpret_cast<u32x4*>(kzs + 8 * v) = u32x4{z[0], z[1], s[0], s[1]};
    *reinterpret_cast<u32x4*>(kzs + 8 * v + 4) = u32x4{z[2], z[3], s[2], s[3]};
  };
  auto put_v = [&](int v, const u32x4& z, const u32x4& s) {
#pragma unroll
    for (int h = 0; h < 2; ++h)
      *reinterpret_cast<u32x4*>(vzs + 8 * v + 4 * h) = u32x4{(z[2 * h] & 0xffffu) | (s[2 * h] << 16), (z[2 * h] >> 16) | (s[2 * h] & 0xffff0000u),
                                                             (z[2 * h + 1] & 0xffffu) | (s[2 * h + 1] << 16), (z[2 * h + 1] >> 16) | (s[2 * h + 1] & 0xffff0000u)};
  };
  unsigned char* slot1 = reinterpret_cast<unsigned char*>(kzs) + 3 * 4096;
  const unsigned short* ksg = reinterpret_cast<const unsigned short*>(ks_);
  const unsigned short* kzg = reinterpret_cast<const unsigned short*>(kz_);
  const unsigned short* vsg = reinterpret_cast<const unsigned short*>(vs_);
  const unsigned short* vzg = reinterpret_cast<const unsigned short*>(vz_);

  if (w == NWAVE) {
    unsigned long long* slot = reinterpret_cast<unsigned long long*>(ws + WS_PACE) + (wg / PGS) * (PGS * PLANE);
    const int me = wg % PGS;
    const __amdgpu_buffer_rsrc_t wrs = __builtin_amdgcn_make_buffer_rsrc((void*)ws, 0, WS_BYTES, 0x00020000);
    const unsigned t0 = (unsigned)lds_ld(org + 2);
    const int mem = wg & 1, xh = (min(ps, 2) * NWG + wg) / 3, xo = WS_X + (xh >> 1) * 32768, xb = l0 + ps * NST;
    int xs = 4, par[2] = {0, 0};
    auto line = [=](const int* t, int k) {
      const unsigned r = (t0 >> 12) * 0x9E3779B1u;
      return reinterpret_cast<const unsigned char*>(t) + ((unsigned)xh * (SKV * DH * 4u) + ((5u * k + (r >> 26)) & 63u) * 32768u + (((k + 1) & 2) << 12) + ((k & 2) << 11) + ((k & 4) << 12) + (((r >> 21) + 11u * k) & 31u) * 128u);
    };
    unsigned long long tag = 0;
    {
      int pre = 0;
      if (ps < 3 && mem && lane < 16) pre = *reinterpret_cast<const int*>(line(lane & 8 ? qv : qk, lane & 7));
      __builtin_amdgcn_sched_barrier(0);
      const int l48 = min(lane, 47), nv = (ps < 3 ? MROW : 3 * NROW) * 8 - 1;
      const unsigned gk = sc_off(512 + l48, DH);
      const u32x4 kz = ldnt4(kzg + gk), ks = ldnt4(ksg + gk);
      u32x4 ql[6], vz[5], vs[5];
#pragma unroll
      for (int i = 0; i < 6; ++i)
        ql[i] = ldnt4(reinterpret_cast<const unsigned short*>(q_) + (unsigned)((((i >> 1) * NWG + wg) / 3) * SQ * DH + 8 * (l48 + 48 * (i & 1))));
#pragma unroll
      for (int i = 0; i < 5; ++i) { const unsigned gv = sc_off(min(256 + 64 * i + lane, nv), GRP); vz[i] = ldnt4(vzg + gv); vs[i] = ldnt4(vsg + gv); }
      tag = __hip_atomic_load(reinterpret_cast<unsigned long long*>(ws + WS_CTL + (min(lane, 2) * NWG + wg) / 3 * 128 + 16), __ATOMIC_RELAXED, __HIP_MEMORY_SCOPE_AGENT);
      u32x2 pt = {0u, 0u};
      if (PT) pt = __builtin_amdgcn_raw_buffer_load_b64(wrs, WS_PACE + ((wg / PGS) * PGS + (lane & 7)) * (PLANE * 8), 0, AUX_SC1);
      unsigned pu0 = 0, pu1 = 0;
      if (PTH & 3) {
        const int y = 2 * ((cp * NWG + wg) / 3), l = PTH & 2 ? lane : (lane & 9) | 6;
        const bool used = (((l >> 1) ^ (l >> 4)) & 3) != 3 || l < 16;
        pu0 = __builtin_amdgcn_raw_buffer_load_b32(wrs, used ? WS_PART + (y >> 5) * 131072 + (y & 31) * 1024 + (l >> 4) * 32768 + (l & 15) * 128 : 1 << 30, 0, AUX_SC1);
      }
      if (PTH & 4) pu1 = reinterpret_cast<const unsigned*>(out)[(cp * NWG + wg) / 3 * 384 + min(lane, 11) * 32];
      __builtin_amdgcn_sched_barrier(0);
      asm volatile("" :: "v"(pre), "v"(cw));
      put_k(512 + l48, kz, ks);
#pragma unroll
      for (int i = 0; i < 6; ++i) *reinterpret_cast<u32x4*>(qs + 8 * (l48 + 48 * i)) = ql[i];
      asm volatile("s_waitcnt lgkmcnt(0)" ::: "memory");
      if (lane == 0) __hip_atomic_fetch_add(rdy, 1, __ATOMIC_RELAXED, __HIP_MEMORY_SCOPE_WORKGROUP);
      __builtin_amdgcn_sched_barrier(0);
#pragma unroll
      for (int i = 0; i < 5; ++i) put_v(min(256 + 64 * i + lane, nv), vz[i], vs[i]);
      asm volatile("s_waitcnt lgkmcnt(0)" ::: "memory");
      if (ps == 3 && lane == 0) __hip_atomic_fetch_add(rdy, 0x100, __ATOMIC_RELAXED, __HIP_MEMORY_SCOPE_WORKGROUP);
      asm volatile("" :: "v"(pt), "v"(pu0), "v"(pu1));
    }
    tag = tag * 0x9E3779B97F4A7C15ull + 0xD1B54A32D192ED03ull;
    const unsigned xlo = __builtin_amdgcn_readlane((unsigned)tag, min(ps, 2)) ^ 0x5bd1e995u, xhi = __builtin_amdgcn_readlane((unsigned)(tag >> 32), min(ps, 2));
    auto xput = [&](int i, int v) { if (lane < 16) __builtin_amdgcn_raw_buffer_store_b64(u32x2{xlo + v, xhi}, wrs, xo + 128 * i + 8 * lane, 0, AUX_SC1); };
    auto xget = [&](int i) {
      asm volatile("" ::: "memory");
      const u32x2 x = __builtin_amdgcn_raw_buffer_load_b64(wrs, xo + 128 * i, 0, FLA);
      asm volatile("" ::: "memory");
      const unsigned d = __builtin_amdgcn_readfirstlane(x[0]) - xlo;
      return __builtin_amdgcn_readfirstlane(x[1]) == xhi && d <= 4u ? (int)d : 0;
    };
    auto flip = [&]() {
#pragma unroll 1
      for (int v = lane; v < NST * 4; v += 64) {
        u32x4* a = reinterpret_cast<u32x4*>(tab + xb * 32) + v; u32x4* b = reinterpret_cast<u32x4*>(tab + NS * 32) + v;
        const u32x4 x = *a, y = *b;
        *a = y; *b = x;
      }
    };
    if (ps < 3) {
      u32x4 vz[2], vs[2], kz[4], ks[4];
#pragma unroll
      for (int i = 0; i < 2; ++i) { const unsigned gv = sc_off(min(576 + 64 * i + lane, MROW * 8 - 1), GRP); vz[i] = ldnt4(vzg + gv); vs[i] = ldnt4(vsg + gv); }
#pragma unroll
      for (int i = 0; i < 4; ++i) { const unsigned o = sc_off(min(1840 + 64 * i + lane, MROW * 24 - 1), DH); kz[i] = ldnt4(kzg + o); ks[i] = ldnt4(ksg + o); }
      __builtin_amdgcn_sched_barrier(0);
      int x = 0;
      if (!mem) {
#pragma unroll 1
        for (int i = 0; i < 2; ++i) {
          int mn[2] = {1 << 20, 1 << 20};
#pragma unroll 1
          for (int k = -1; k < 2 * NSP; ++k) {
            int d = load_time(line(i ? qv : qk, k & 7));
            if (k < 0 || d < 270) d = 1 << 20;
            if ((k + 1) & 2) mn[1] = min(mn[1], d); else mn[0] = min(mn[0], d);
          }
          x += (FORCE ? (FORCE >> i) & 1 : mn[1] < mn[0]) << i;
        }
        xput(0, 4 - x);
      }
#pragma unroll
      for (int i = 0; i < 2; ++i) put_v(min(576 + 64 * i + lane, MROW * 8 - 1), vz[i], vs[i]);
#pragma unroll
      for (int i = 0; i < 4; ++i) put_k(min(1840 + 64 * i + lane, MROW * 24 - 1), kz[i], ks[i]);
      if (lds_ld(pprog) >= 8 && lane == 0) org[1] = 0;
      if (mem) {
        for (int sp = 0; sp < (1 << 20) && !(x = xget(0)); ++sp) if (lds_ld(pprog) >= 8 && lane == 0) org[1] = 0;
        --x;
      }
      par[0] = x & 1; par[1] = x >> 1;
      for (int sp = 0; sp < (1 << 22) && ((lds_ld(rdy) >> 8) & 255) < NWAVE; ++sp) __builtin_amdgcn_s_sleep(1);
      asm volatile("" ::: "memory");
      if (par[0]) flip();
      asm volatile("s_waitcnt lgkmcnt(0)" ::: "memory");
      if (lane == 0) {
        __hip_atomic_fetch_add(rdy, 0x100, __ATOMIC_RELAXED, __HIP_MEMORY_SCOPE_WORKGROUP);
        if (par[0] == par[1]) __hip_atomic_store(xfl, 1, __ATOMIC_RELAXED, __HIP_MEMORY_SCOPE_WORKGROUP);
      }
      if (par[0] != par[1]) xs = 1;
    }
    constexpr int PST = HL ? 768 : SQ * DH * 4, PNX = HL ? 1024 : 4096;
    auto pslot = [](int y) { return HL ? WS_PART + (y >> 5) * 131072 + (y & 31) * 1024 : WS_PART + y * 4096; };
    int pln = HL ? g * 32768 + g * 256 + 16 * m : 16 * lane;
    auto pu = [](int a, int u) { return HL ? a ^ (u << 8) : a + 1024 * u; };
    auto part = [&](int t) { const int p = (t + 3 - wg % 3) % 3, h = (p * NWG + wg) / 3; return pslot(2 * h + (p == ps ? par[1] : t)); };
    int vp0 = part(0) + pln, vp1 = part(1) + pln, vf = (lane & 16 ? part(1) : part(0)) + PST + 8 * m;
    int lz = (lane & 16) * 256 + SQ * DH * 4 + 8 * (lane & 3);
    const unsigned long long tg = __shfl(tag, (g + 3 - wg % 3) % 3);
    u32x2 tgv = {(unsigned)tg, (unsigned)(tg >> 32)};
    const int ch = (cp * NWG + wg) / 3;
    unsigned ctlo = __builtin_amdgcn_readlane((unsigned)tag, cp), cthi = __builtin_amdgcn_readlane((unsigned)(tag >> 32), cp);
    int so = pslot(2 * ch);
    int fw = so + (lane & 1) * PNX + PST + 128, eo = WS_CTL + ch * 128 + 16, oo = ch * (SQ * DH * 2) + 8 * lane;
    asm volatile("" : "+v"(vp0), "+v"(vp1), "+v"(vf), "+v"(lz), "+v"(tgv), "+v"(fw), "+v"(eo), "+v"(oo));
    asm volatile("" : "+s"(ctlo), "+s"(cthi), "+s"(so));
    auto xadr = [=](int who, int i) {
      const int y = xh / (SHM / 6) + who;
      return XL ? WS_Y + (y >> 3) * 131072 + (y & 7) * 4096 + g * 32768 + ((i ^ g) << 8) + 16 * m : xo + 1024 + who * 12288 + 16 * lane + 1024 * i;
    };
    auto xvec = [=](int v, const float* mx) {
      const int ww = v / 86;
      return v < 688 ? reinterpret_cast<unsigned char*>(pl) + ww * (NS * 32) + xb * 32 + 16 * (v - 86 * ww) : (unsigned char*)mx;
    };
    unsigned xt = 0;
    auto xstep = [&]() {
      const unsigned t = (unsigned)__builtin_amdgcn_s_memrealtime();
      if (xs == 1) {
        if (((lds_ld(rdy) >> (ps < 2 ? 24 : 16)) & 255) < NWAVE) return;
        asm volatile("" ::: "memory");
        xs = 2; xt = XQ ? (t + 150 + XQ) & -XQ : t;
      }
      if ((int)(xt - t) > 150) return;
      int a = xadr(xs == 2 ? mem : mem ^ 1, 0);
      u32x4 y[11];
      if (xs == 2) {
#pragma unroll
        for (int i = 0; i < 11; ++i) { y[i] = *reinterpret_cast<const u32x4*>(xvec(lane + 64 * i, stats + (ps * NWAVE + (lane & 7)) * 8)); asm volatile("" : "+v"(y[i])); }
        if (XFL && lane == 0) org[1] = 1;
        wait_tv<63>(xt, 1);
#pragma unroll
        for (int i = 0; i < 11; ++i) __builtin_amdgcn_raw_buffer_store_b128(y[i], wrs, a ^ (i << 8), 0, XAUX);
        asm volatile("s_waitcnt vmcnt(0)" ::: "memory");
        xput(1 + mem, 1);
        xs = 3; xt += XQ ? XQ / 2 : 400;
        flip();
      } else {
        if (!xget(2 - mem)) { xt += XQ ? XQ : 100; return; }
        wait_tv<63>(xt, 1);
#pragma unroll
        for (int i = 0; i < 11; ++i) y[i] = __builtin_amdgcn_raw_buffer_load_b128(wrs, a ^ (i << 8), 0, XLA);
#pragma unroll
        for (int i = 0; i < 11; ++i) *reinterpret_cast<u32x4*>(xvec(lane + 64 * i, xmax + 4 * (lane & 7))) = y[i];
        xs = 4;
        asm volatile("s_waitcnt lgkmcnt(0)" ::: "memory");
        if (lane == 0) __hip_atomic_store(xfl, 3, __ATOMIC_RELAXED, __HIP_MEMORY_SCOPE_WORKGROUP);
        if (XFL && lane == 0) org[1] = 0;
      }
    };
    int last = 0, last8 = 0, nck = CK0, want = 0, own = 0, po = 0, rate = 0, left = 0;
    bool got = true;
    float* park = reinterpret_cast<float*>(kzs);
    int qv3[3];
#pragma unroll
    for (int u = 0; u < 3; ++u) qv3[u] = (lane + 64 * u) / 48;
    constexpr int TOTAL = 2 * NPR;
    int nred = 0;
    for (int spin = 0; nred < 3 && spin < (1 << 23); ++spin) {
      const int n8 = lds_ld(pprog), n = n8 >> 3;
      if (n8 != last8 && n < TOTAL - 4) {
        if (left > 0) {
          const int q = min(n8 - last8, left);
          if (lane == 0) __hip_atomic_fetch_add(org, q * rate, __ATOMIC_RELAXED, __HIP_MEMORY_SCOPE_WORKGROUP);
          left -= q;
          if (left == 0 && lane == 0) org[1] = 0;
        }
        last8 = n8;
      }
      if (last < 1 && n >= 1 && lane == 0) org[1] = 0;
      if (xs < 4) xstep();
      if (n != last && n < TOTAL - 4) {
        if (n >= nck) {
          own = lds_ld(org) >> 8;
          if (lane == 0)
            __hip_atomic_store(slot + me * PLANE, ((unsigned long long)nck << 32) | (t0 + (unsigned)own), __ATOMIC_RELAXED, __HIP_MEMORY_SCOPE_AGENT);
          want = nck; nck = nck == CK0 ? CK1 : nck + PCK; left = 0;
          if (lane == 0) org[1] = 0;
          got = lane == me || lane >= PGS; po = 0;
        }
        if (want && n >= want + PDLY) {
          if (!got) {
            asm volatile("" ::: "memory");
            const u32x2 xx = __builtin_amdgcn_raw_buffer_load_b64(wrs, WS_PACE + ((wg / PGS) * PGS + lane) * (PLANE * 8), 0, PLA);
            const unsigned long long x = xx[0] | ((unsigned long long)xx[1] << 32);
            const int r = (int)((unsigned)x - t0) - own;
            if ((int)(x >> 32) == want && r > -2000 && r < 2000) { got = true; po = r; }
          }
          int e = got ? po : PDLY * 2 * TAU / 10;
#pragma unroll
          for (int k = 1; k < PGS; k <<= 1) e += __shfl_xor(e, k);
          e = __builtin_amdgcn_readfirstlane(e);
          rate = min(max(want == CK0 ? e * 32 / (PGS * SPAN0) : e * 32 / (PGS * SPAN), -RDN * 32), RUP * 32);
          left = want == CK0 ? 8 * SPAN0 : 8 * SPAN; want = 0;
          if (lane == 0) org[1] = rate < -64;
        }
        last = n;
      }
      if ((lds_ld(cnt + nred) | (nred == 2 ? 254 : 0)) == 255) {
        for (int sp = 0; sp < (1 << 22) && ((lds_ld(rdy) >> 16) & 255) < NWAVE; ++sp) __builtin_amdgcn_s_sleep(1);
        asm volatile("" ::: "memory");
        const int pm = (ord >> (2 * nred)) & 3;
        const int t = (pm + wg) % 3;
        const float* st = stats + pm * NWAVE * 8;
        const unsigned char* sl = (nred & 1 ? slot1 : kst) + 16 * lane;
        f32x4 o[3]; float mx[3], sm[3], f[3][NWAVE];
#pragma unroll
        for (int u = 0; u < 3; ++u) {
          mx[u] = -INFINITY;
#pragma unroll
          for (int ww = 0; ww < NWAVE; ++ww) { f[u][ww] = st[ww * 8 + qv3[u]]; mx[u] = fmaxf(mx[u], f[u][ww]); }
#pragma unroll
          for (int ww = 0; ww < NWAVE; ++ww) { f[u][ww] = __expf(f[u][ww] - mx[u]); asm volatile("" : "+v"(f[u][ww])); }
          o[u] = f32x4{0.f, 0.f, 0.f, 0.f}; sm[u] = 0.f;
        }
#pragma unroll
        for (int w2 = 0; w2 < NWAVE; w2 += 2) {
          f32x4 y[2][3]; float z[2][3];
          for (int sp = 0; sp < (1 << 22); ++sp) {
            const int c = __hip_atomic_load(cnt + nred, __ATOMIC_RELAXED, __HIP_MEMORY_SCOPE_WORKGROUP);
            asm volatile("" ::: "memory");
#pragma unroll
            for (int k = 0; k < 2; ++k)
#pragma unroll
              for (int u = 0; u < 3; ++u) {
                y[k][u] = *reinterpret_cast<const f32x4*>(sl + (w2 + k) * OSL + 1024 * u);
                z[k][u] = st[(w2 + k) * 8 + 4 + qv3[u]];
              }
            asm volatile("" ::: "memory");
            if (((__builtin_amdgcn_readfirstlane(c) >> w2) & 3) == 3) break;
          }
#pragma unroll
          for (int k = 0; k < 2; ++k)
#pragma unroll
            for (int u = 0; u < 3; ++u) { o[u] += f[u][w2 + k] * y[k][u]; sm[u] += f[u][w2 + k] * z[k][u]; }
          asm volatile("" : "+v"(o[0]), "+v"(o[1]), "+v"(o[2]), "+v"(sm[0]), "+v"(sm[1]), "+v"(sm[2]));
        }
#pragma unroll
        for (int u = 0; u < 3; ++u) {
          *reinterpret_cast<f32x4*>(park + t * 1024 + 4 * (lane + 64 * u)) = o[u];
          *reinterpret_cast<f32x2*>(park + t * 1024 + SQ * DH + 2 * qv3[u]) = f32x2{mx[u], sm[u]};
        }
        ++nred;
        asm volatile("s_waitcnt lgkmcnt(0)" ::: "memory");
        if (lane == 0) __hip_atomic_store(mrg, nred, __ATOMIC_RELAXED, __HIP_MEMORY_SCOPE_WORKGROUP);
        continue;
      }
      __builtin_amdgcn_s_sleep(4);
    }
    {
      u32x4 y[2][3];
#pragma unroll
      for (int t = 0; t < 2; ++t)
#pragma unroll
        for (int u = 0; u < 3; ++u) y[t][u] = *reinterpret_cast<const u32x4*>(park + t * 1024 + 4 * (lane + 64 * u));
      u32x2 z = *reinterpret_cast<const u32x2*>(reinterpret_cast<const unsigned char*>(park) + lz);
      asm volatile("" : "+v"(z));
#pragma unroll
      for (int t = 0; t < 2; ++t)
#pragma unroll
        for (int u = 0; u < 3; ++u) __builtin_amdgcn_raw_buffer_store_b128(y[t][u], wrs, pu(t ? vp1 : vp0, u), 0, HSA);
      if (lane < 32) __builtin_amdgcn_raw_buffer_store_b64(z, wrs, vf, 0, HSA);
      asm volatile("s_waitcnt vmcnt(0)" ::: "memory");
      if (lane < 32) __builtin_amdgcn_raw_buffer_store_b64(tgv, wrs, vf + 128, 0, AUX_SC1);
    }
    {
      f32x4 x[2][3], ro[3]; u32x2 ms[2][3]; float rm[3], rs[3];
#pragma unroll
      for (int u = 0; u < 3; ++u) {
        ro[u] = *reinterpret_cast<const f32x4*>(park + 2 * 1024 + 4 * (lane + 64 * u));
        const f32x2 a = *reinterpret_cast<const f32x2*>(park + 2 * 1024 + SQ * DH + 2 * qv3[u]);
        rm[u] = a[0]; rs[u] = a[1];
      }
      const float* st = stats + 3 * NWAVE * 8;
      auto try3 = [&](int ww) {
        const int c = __hip_atomic_load(cnt + 3, __ATOMIC_RELAXED, __HIP_MEMORY_SCOPE_WORKGROUP);
        asm volatile("" ::: "memory");
        f32x4 y[3];
#pragma unroll
        for (int u = 0; u < 3; ++u) y[u] = *reinterpret_cast<const f32x4*>(slot1 + ww * OSL + 16 * (lane + 64 * u));
        asm volatile("" ::: "memory");
        const int ok = (__builtin_amdgcn_readfirstlane(c) >> ww) & 1;
        if (ok) {
#pragma unroll
          for (int u = 0; u < 3; ++u) ro[u] += y[u];
        }
        return ok;
      };
      constexpr int TB = 4;
      int nw = 0;
#pragma unroll 1
      for (int spin = 0; spin < (1 << 21); ++spin) {
        asm volatile("" ::: "memory");
        const u32x2 fl = __builtin_amdgcn_raw_buffer_load_b64(wrs, fw, 0, FLA);
        asm volatile("" ::: "memory");
#pragma unroll 1
        for (int k = 0; k < 4 && nw < NWAVE - TB && try3(nw); ++k) ++nw;
        if (__all(fl[0] == ctlo && fl[1] == cthi)) break;
        __builtin_amdgcn_s_sleep(1);
      }
      asm volatile("" ::: "memory");
#pragma unroll
      for (int t = 0; t < 2; ++t)
#pragma unroll
        for (int u = 0; u < 3; ++u) {
          x[t][u] = __builtin_bit_cast(f32x4, __builtin_amdgcn_raw_buffer_load_b128(wrs, pu(pln, u), so + t * PNX, HLA));
          ms[t][u] = __builtin_amdgcn_raw_buffer_load_b64(wrs, PST + 8 * qv3[u], so + t * PNX, HLA);
        }
      __builtin_amdgcn_sched_barrier(0);
      for (int q = 0; q < (1 << 22) && ((lds_ld(cnt + 3) >> 8) & 255) != 255; ++q) __builtin_amdgcn_s_sleep(1);
      asm volatile("" ::: "memory");
#pragma unroll
      for (int u = 0; u < 3; ++u)
#pragma unroll
        for (int ww = 0; ww < NWAVE; ++ww) rs[u] += st[ww * 8 + 4 + qv3[u]];
      asm volatile("" : "+v"(rs[0]), "+v"(rs[1]), "+v"(rs[2]));
#pragma unroll 1
      for (int spin = 0; spin < (1 << 22) && nw < NWAVE - TB; ++spin) nw += try3(nw);
      __builtin_amdgcn_sched_barrier(0);
      float fc[3];
#pragma unroll
      for (int u = 0; u < 3; ++u) {
        const float m0 = __uint_as_float(ms[0][u][0]), m1 = __uint_as_float(ms[1][u][0]);
        const float mx = fmaxf(rm[u], fmaxf(m0, m1));
        const float f0 = __expf(m0 - mx), f1 = __expf(m1 - mx), f2 = __expf(rm[u] - mx);
        const float r = 1.f / (f0 * __uint_as_float(ms[0][u][1]) + f1 * __uint_as_float(ms[1][u][1]) + f2 * rs[u]);
        fc[u] = f2 * r;
        x[0][u] = (f0 * r) * x[0][u] + (f1 * r) * x[1][u];
        asm volatile("" : "+v"(x[0][u]), "+v"(fc[u]));
      }
      __builtin_amdgcn_sched_barrier(0);
      const __amdgpu_buffer_rsrc_t ors = __builtin_amdgcn_make_buffer_rsrc((void*)out, 0, NWG * SQ * DH * 2, 0x00020000);
      {
        f32x4 y[TB][3];
        for (int sp = 0; sp < (1 << 22); ++sp) {
          const int c = __hip_atomic_load(cnt + 3, __ATOMIC_RELAXED, __HIP_MEMORY_SCOPE_WORKGROUP);
          asm volatile("" ::: "memory");
#pragma unroll
          for (int u = 0; u < 3; ++u)
#pragma unroll
            for (int k = 0; k < TB; ++k) y[k][u] = *reinterpret_cast<const f32x4*>(slot1 + (NWAVE - TB + k) * OSL + 16 * (lane + 64 * u));
          asm volatile("" ::: "memory");
          if ((__builtin_amdgcn_readfirstlane(c) & 255) == 255) break;
        }
#pragma unroll
        for (int u = 0; u < 3; ++u) {
#pragma unroll
          for (int k = 0; k < TB; ++k) ro[u] += y[k][u];
          const f32x4 o = x[0][u] + fc[u] * ro[u];
          __builtin_amdgcn_raw_buffer_store_b64(u32x2{cvtpk(o[0], o[1]), cvtpk(o[2], o[3])}, ors, oo + 512 * u, 0, OSA);
        }
        if (lane == 0) __builtin_amdgcn_raw_buffer_store_b64(u32x2{ctlo, cthi}, wrs, eo, 0, AUX_SC1);
      }
    }
    asm volatile(".Lkivi_code_b:");
    return;
  }

  u32x4 sz[4], ss[4];
  const int vb = (w < 2 ? 256 * w : (w < 6 ? 256 * w + 48 : (w == 7 ? 1584 : 0))) + lane;
  const bool hasc = w < 7 || ps < 3;
  {
    const unsigned short* zg = w != 6 ? kzg : vzg;
    const unsigned short* sg = w != 6 ? ksg : vsg;
    unsigned o[4];
#pragma unroll
    for (int i = 0; i < 4; ++i) o[i] = sc_off(vb + 64 * i, w != 6 ? DH : GRP);
    if (hasc) {
#pragma unroll
      for (int i = 0; i < 4; ++i) { sz[i] = ldnt4(zg + o[i]); ss[i] = ldnt4(sg + o[i]); }
    } else {
#pragma unroll
      for (int i = 0; i < 4; ++i) asm volatile("" : "=v"(sz[i]), "=v"(ss[i]));
    }
    asm volatile("" ::: "memory");
    __builtin_amdgcn_sched_barrier(0);
  }
  const __amdgpu_buffer_rsrc_t krs = __builtin_amdgcn_make_buffer_rsrc((void*)qk, 0, (int)TBYTES, 0x00020000);
  const __amdgpu_buffer_rsrc_t vrs = __builtin_amdgcn_make_buffer_rsrc((void*)qv, 0, (int)TBYTES, 0x00020000);
  const float scale = 0.07216878364870322f;
  unsigned char* myst = kst + w * OSL;
  unsigned short* mypl = pl + w * (NS * 16) + 4 * (m & 3) + g;
  const unsigned short* mytab = tab + w * 4 + g;
  const int mseg = m >> 2;
  unsigned mb = frow(mseg) * GRP;
  unsigned msz = mseg == 3 ? 0 : (mseg == ps ? NROW + XROW : NROW) * GRP;
  auto sbase = [=](int p) { return (unsigned)__builtin_amdgcn_readfirstlane(gbase(p) * (GRP * DH * 4)); };
  const unsigned sb0 = sbase(0), sb1 = sbase(1), sb2 = sbase(2);
  auto soff = [=](int i) { unsigned r = pick((unsigned)(i - l0) < (unsigned)NST, sb0, pick(i < 2 * NST, sb1, sb2)); asm volatile("" : "+s"(r)); return r; };
  auto voff = [&](int e) { int v = (e & 0x7fff) * (DH * 4) + 16 * m; asm volatile("" : "+v"(v)); return v; };
  auto issue = [&](const __amdgpu_buffer_rsrc_t& rs, i32x4 (&x)[3], int vo, unsigned so) {
#pragma unroll
    for (int u = 0; u < 3; ++u) x[u] = ldbuf(rs, vo + 256 * u, so);
  };

  constexpr int T256 = TAU * 256 / 10;
  unsigned tbase;
  auto now = [&]() { return (int)((unsigned)__builtin_amdgcn_s_memrealtime() - tbase); };
  auto grid = [&](int k) { return ((8 * k + w) * (T256 / 8)) >> 8; };
  auto wait_t = [&](int rel) {
    __builtin_amdgcn_sched_barrier(0);
    for (;;) {
      const int d = rel - now();
      if (d <= 0) break;
      if (d > 12) __builtin_amdgcn_s_sleep(2);
    }
    __builtin_amdgcn_sched_barrier(0);
  };
  int go = 0, grel = 0;
  auto gate = [&](int k) {
    const int rel = grid(k);
    __builtin_amdgcn_sched_barrier(0);
    const unsigned long long of = __hip_atomic_load(reinterpret_cast<unsigned long long*>(org), __ATOMIC_RELAXED, __HIP_MEMORY_SCOPE_WORKGROUP);
    int t = now();
    int o = __builtin_amdgcn_readfirstlane((int)of) >> 8;
    const int fl = __builtin_amdgcn_readfirstlane((int)(of >> 32));
    while (o + rel - t > 18) { __builtin_amdgcn_s_sleep(2); o = lds_ld(org) >> 8; t = now(); }
    go = o; grel = rel + GLATE;
    __builtin_amdgcn_sched_barrier(0);
    if (ALLFL || fl) wait_tv<3>(tbase + (unsigned)(o + rel), o + rel - t);
    else wait_tv<0>(tbase + (unsigned)(o + rel), o + rel - t);
    __builtin_amdgcn_sched_barrier(0);
  };
  auto stamp = [&]() {
    __builtin_amdgcn_sched_barrier(0);
    const int o = now() - grel;
    if (o > go) lds_max(org, o << 8);
    __builtin_amdgcn_sched_barrier(0);
  };

  i32x4 xa[3], xb[3], xc[3], xd[3];
  const int s0 = ord == 0x09 ? 2 * NST - RS : NST - (cp == 0 ? RS : 0), s1 = cp == 2 ? NS - 2 * RS : 2 * NST - RS;
  auto vidx_f = [=](int j) {
    int s = j;
    if (j >= NST - RS && j < NST && (cp == 0 || ps == 1)) s += NST;
    if (j >= 2 * NST - RS && j < 2 * NST) s += NST;
    if (cp == 2) { if (j >= NS - 2 * RS) s += j < NS - RS ? RS - (ps == 1 ? 2 : 1) * NST : -RS; }
    else if (j >= NS - RS) s += (cp - 2) * NST;
    return s < NST ? s + l0 : (s < NST + l0 ? s - NST : s);
  };
  int vperm = vidx_f(lane) | (vidx_f(64 + lane) << 8) | ((vidx_f(128 + lane) & 255) << 16);
  asm volatile("" : "+v"(vperm));
  const unsigned tb0 = (unsigned)lds_ld(org + 2);
#pragma unroll
  for (int i = 0; i < 3; ++i) {
    const int s = lane + 64 * i, p = s >= 2 * NST ? 2 : ((unsigned)(s - l0) < (unsigned)NST ? 0 : 1);
    int n = NWAVE * (s % NST) + w + SLIP * (wg % SLM);
    if (n >= NQ) n -= NQ;
    const int b = n / NST, add = lrow(p) * GRP;
    u32x2 e;
    e = u32x2{0u, 0u};
#pragma unroll
    for (int r = 0; r < 4; ++r) {
      const int q = 2 * (n - NST * b) + (r & 1), j = q < NST ? q : q - NST, S = 32 * ((p + wg) % 3) + 4 * b + (q < NST ? 0 : 2) + (r >> 1);
      const int k0 = (128 * S + 2) / 3, sh = 3 * k0 - 128 * S == 2;
      const int k = r < 2 ? (sh && j == 0 ? k0 + 0x8000 : k0 + j - sh) : (sh && j == NST - 1 ? k0 + j - 1 + 0x8000 : k0 + j);
      e[r >> 1] |= (unsigned)(k + add) << (16 * (r & 1));
    }
    if (s < NS && p != ps) *reinterpret_cast<u32x2*>(tab + s * 32 + w * 4) = e;
  }
  if (ps < 3 && w >= 4) {
    unsigned short* tb = tab + (w & 2 ? NS : l0 + ps * NST) * 32 + 2 * (w & 1);
    const int sg = 8 * (lane >> 2) + 64 * ((lane ^ w) & 2) + 4 * (w & 1) + 2 * (lane & 1) + ((w >> 1) & 1), add = lrow(ps) * GRP;
    const int k0 = (32 * sg + 1) / 3 + add, k1 = (32 * sg + 33) / 3 + add;
    const int i0 = 11 * lane - __popcll(__ballot(k1 - k0 == 10) & ((1ull << lane) - 1));
    int q = i0 + i0 / 137, r = i0 % 137;
    for (int k = k0; k < k1; ++k, ++q) {
      tb[(q >> 1) * 4 + (q & 1)] = (unsigned short)k;
      if (++r == 137) { tb[(q >> 1) * 4 + 1] = (unsigned short)(k + 0x8000); ++q; }
    }
    if (lane == 63) for (; q < 2 * NQ; ++q) tb[(q >> 1) * 4 + (q & 1)] = (unsigned short)(k1 - 1 + 0x8000);
  }
  asm volatile("" ::: "memory");
  int E0 = mytab[0], E1 = mytab[32], E2 = mytab[64];
  const int v0 = voff(E0), v1 = voff(E1);
  asm volatile("" : "+v"(E0), "+v"(E1), "+v"(E2));
  int Ea = 0, Eb = 0, Ec = 0, vo1 = 0;
  auto prologue = [&]() {
    tbase = tb0;
    Ea = E0; Eb = E1; Ec = E2; vo1 = v1;
    __builtin_amdgcn_sched_barrier(0);
    auto puts = [&]() {
      if (w != 6) {
#pragma unroll
        for (int i = 0; i < 4; ++i) put_k(vb + 64 * i, sz[i], ss[i]);
      } else {
#pragma unroll
        for (int i = 0; i < 4; ++i) put_v(vb + 64 * i, sz[i], ss[i]);
      }
      asm volatile("s_waitcnt lgkmcnt(0)" ::: "memory");
      if (lane == 0) __hip_atomic_fetch_add(rdy, w < 2 ? 0x101 : 0x100, __ATOMIC_RELAXED, __HIP_MEMORY_SCOPE_WORKGROUP);
    };
    if (w < 2) puts();
    asm volatile("" :: "v"(cw));
    wait_t(ORG0 + grid(0));
    issue(krs, xa, v0, soff(0));
    __builtin_amdgcn_sched_barrier(0);
    if (w >= 2 && hasc) puts();
  };
  int woff[3];
#pragma unroll
  for (int u = 0; u < 3; ++u) {
    const int qq = 8 * u + (m >> 1);
    woff[u] = qq * 128 + (((2 * g) ^ qq) & 7) * 16 + (m & 1) * 8;
  }
  const int roff = g * 128 + ((2 * (m >> 2) + (m & 1)) ^ g) * 16;
  auto vidx = [&](int j) { return (__builtin_amdgcn_readlane(vperm, j & 63) >> (8 * (j >> 6))) & 255; };

  bf16x8 qf[6];
  u32x4 zs0[3];
  float mrun = -INFINITY;
  int Ep0 = 0x8000, Ep1 = 0x8000;
  auto kzread = [&](u32x4 (&zs)[3], int e) {
    const unsigned* zp = kzs + ((e >> 6) & 0x1ff) * DH + 4 * m;
#pragma unroll
    for (int u = 0; u < 3; ++u) zs[u] = *reinterpret_cast<const u32x4*>(zp + 64 * u);
  };
  auto kdeq = [&](const i32x4 (&x)[3], const u32x4 (&zs)[3], int c) {
#pragma unroll
    for (int u = 0; u < 3; ++u) {
      const i32x4 y = x[u];
      u32x2 o = {deq2(y[0], y[1], zs[u][0], zs[u][2]), deq2(y[2], y[3], zs[u][1], zs[u][3])};
      *reinterpret_cast<u32x2*>(myst + (woff[u] ^ (c << 4))) = o;
    }
  };
  auto kread = [&](u32x4 (&a)[6]) {
#pragma unroll
    for (int j = 0; j < 6; ++j) a[j] = *reinterpret_cast<const u32x4*>(myst + ((roff + 512 * j) ^ ((j & 1) << 6)));
  };
  auto kfin = [&](const u32x4 (&a)[6], int e0, int e1, int i) {
    f32x4 acc = {0.f, 0.f, 0.f, 0.f};
#pragma unroll
    for (int j = 0; j < 6; ++j) acc = __builtin_amdgcn_mfma_f32_16x16x32_bf16(__builtin_bit_cast(bf16x8, a[j]), qf[j], acc, 0, 0, 0);
    const unsigned o = cvtpk(bfround(acc[0]) * scale, bfround(acc[1]) * scale);
    if ((unsigned)e0 - mb < msz) { mrun = fmaxf(mrun, bflo(o)); mypl[i * 16] = (unsigned short)o; }
    if ((unsigned)e1 - mb < msz) { mrun = fmaxf(mrun, bfhi(o)); mypl[i * 16 + 16] = (unsigned short)(o >> 16); }
  };
  auto kissue = [&](i32x4 (&x)[3], int vo, unsigned so) { issue(krs, x, vo, so); stamp(); };
#pragma unroll
  for (int j = 0; j < 6; ++j) asm volatile("" : "=v"(qf[j]));
#pragma unroll
  for (int u = 0; u < 3; ++u) asm volatile("" : "=v"(zs0[u]));
  auto adopt = [&]() {
    int x = 0;
    for (int spin = 0; spin < (1 << 22) && !(x = lds_ld(xfl)); ++spin) __builtin_amdgcn_s_sleep(1);
    asm volatile("" ::: "memory");
    if ((x & 2) && mseg == ps) { mrun = xmax[4 * w + (m & 3)]; stats[(ps * NWAVE + w) * 8 + (m & 3)] = mrun; }
  };
  static_assert(LD % 2 == 0 && LD > 4 && LD <= NST - RS, "");
  const unsigned long long spec = 1ull | (1ull << (ps ? 8 : LD / 2 - 2)) | (ps == 0 ? (1ull << (LD / 2 + 23)) | (1ull << 60) : 0ull) | (ps == 1 ? 1ull << 44 : 0ull);
  prologue();
  {
#pragma unroll 1
    for (int i = 0; i < NS - 1; i += 2) {
      const unsigned so1 = soff(i + 1), so2 = soff(i + 2);
      gate(i + 1);
      kissue(xb, vo1, so1);
      if ((spec >> (i >> 1)) & 1) {
        if (i < 40) {
          const int sh = i ? 8 : 0, need = i ? (ps < 3 ? NWAVE + 1 : NWAVE) : 3;
          for (int spin = 0; spin < (1 << 22) && ((lds_ld(rdy) >> sh) & 255) < need; ++spin) __builtin_amdgcn_s_sleep(1);
          asm volatile("" ::: "memory");
          if (i == 0) {
            kzread(zs0, Ea);
#pragma unroll
            for (int j = 0; j < 6; ++j)
              qf[j] = *reinterpret_cast<const bf16x8*>(qs + min(m, 3 * SQ - 1) * DH + 32 * j + 8 * g);
          }
        } else if (i < 100) {
          mrun = rows<true>(mrun);
          if (lane < 2 * SQ) stats[(mseg * NWAVE + w) * 8 + (m & 3)] = mrun;
          lds_add(rdy, 1 << 24);
        } else adopt();
      }
      u32x4 a[6], zs1[3];
      kread(a);
      kzread(zs1, Eb);
      const int in = i + 3 < NS ? i + 3 : l0;
      const int En0 = mytab[in * 32], En1 = mytab[in * 32 + 32];
      kdeq(xa, zs0, 0);
      kfin(a, Ep0, Ep1, i - 2);
      const int vo2 = voff(Ec);
      gate(i + 2);
      kissue(xa, vo2, so2);
      kzread(zs0, Ec);
      kdeq(xb, zs1, 1);
      lds_add(pprog, 1);
      Ep0 = Ea; Ep1 = Eb; Ea = Ec; Eb = En0; Ec = En1;
      vo1 = voff(Eb);
    }
  }
  f32x4 oacc[3][4];
  float srun;
  unsigned z0;
  auto vdeq = [&](const i32x4& y, unsigned zs, float (&f)[4]) {
    const float z = bflo(zs), s = bfhi(zs);
    const unsigned t0 = cvtpk((float)y[0] - z, (float)y[1] - z), t1 = cvtpk((float)y[2] - z, (float)y[3] - z);
    f[0] = bflo(t0) * s; f[1] = bfhi(t0) * s; f[2] = bflo(t1) * s; f[3] = bfhi(t1) * s;
  };
  auto prob = [&](int e, unsigned lg) { return __expf(__uint_as_float((unsigned)e - mb < msz ? lg << 16 : 0xff800000u) - mrun); };
  {
    gate(NS);
    issue(vrs, xb, vo1, sb0);
    stamp();
    u32x4 a[6];
    kread(a);
    const int En0 = mytab[l0 * 32 + 64], En1 = mytab[l0 * 32 + 96];
    const unsigned zv = vzs[Eb & 0x7fff], lg = mypl[l0 * 16];
    z0 = vzs[Ec & 0x7fff];
    kdeq(xa, zs0, 0);
    kfin(a, Ep0, Ep1, NS - 3);
    const int vo2 = voff(Ec);
    gate(NS + 1);
    issue(vrs, xa, vo2, sb0);
    stamp();
    kread(a);
    unsigned bfr[3][4];
#pragma unroll
    for (int db = 0; db < 3; ++db) {
      float f[4];
      vdeq(xb[db], zv, f);
#pragma unroll
      for (int c = 0; c < 4; ++c) bfr[db][c] = cvtpk(f[c], 0.f);
      if (db == 0) { asm volatile("" : "+v"(bfr[0][0]), "+v"(bfr[0][1]), "+v"(bfr[0][2]), "+v"(bfr[0][3])); }
    }
    kfin(a, Ea, 0x8000, NS - 1);
    mrun = rows<true>(mrun);
    if (lane < 3 * SQ) stats[(mseg * NWAVE + w) * 8 + (m & 3)] = mrun;
    lds_add(rdy, 0x10000);
    if (mseg == 3) mrun = 0.f;
    srun = prob(Eb, lg);
    const u32x2 pa = {cvtpk(srun, 0.f), 0u};
#pragma unroll
    for (int db = 0; db < 3; ++db)
#pragma unroll
      for (int c = 0; c < 4; ++c)
        oacc[db][c] = __builtin_amdgcn_mfma_f32_16x16x16bf16_1k(__builtin_bit_cast(bf16x4, pa), __builtin_bit_cast(bf16x4, u32x2{bfr[db][c], 0u}), f32x4{0.f, 0.f, 0.f, 0.f}, 0, 0, 0);
    lds_add(pprog, 2);
    Ea = Ec; Eb = En0; Ec = En1;
    vo1 = voff(Eb);
  }

  auto vend = [&](int p, int k) {
    const int pm = (ord >> (2 * p)) & 3;
    float* oslot = reinterpret_cast<float*>(p & 1 ? slot1 + w * OSL : myst);
    if (g == pm) {
#pragma unroll
      for (int db = 0; db < 3; ++db)
        if (k >> db & 1) {
#pragma unroll
          for (int r = 0; r < 4; ++r)
            *reinterpret_cast<f32x4*>(oslot + r * DH + 64 * db + 4 * m) = f32x4{oacc[db][0][r], oacc[db][1][r], oacc[db][2][r], oacc[db][3][r]};
          asm volatile("" ::: "memory");
        }
    }
    if (k & 8) {
      const float sr = rows<false>(srun);
      if (g == pm && mseg == pm) stats[(pm * NWAVE + w) * 8 + 4 + (m & 3)] = sr;
      lds_add(cnt + p, 1 << w);
    }
  };
  int El = 0, Ia = l0 + 1, Ib = l0 + 2, Ic = l0 + 3;
  unsigned so1 = sb0;
  auto vpair = [&](auto last, int j) {
    constexpr bool LAST = decltype(last)::value;
    gate(NS + j + 1);
    issue(vrs, xb, vo1, so1);
    stamp();
    const unsigned z1 = vzs[Eb & 0x7fff], lg0 = mypl[Ia * 16], lg1 = mypl[Ib * 16];
    const unsigned so2 = soff(Ic);
    Ia = Ic; Ib = vidx(j + 3); Ic = vidx(j + 4); so1 = soff(Ib);
    const int En0 = mytab[Ib * 32], En1 = mytab[Ic * 32];
    if constexpr (LAST) El = mytab[vidx(j + 5) * 32];
    float f[3][4];
#pragma unroll
    for (int db = 0; db < 3; ++db) {
      vdeq(xa[db], z0, f[db]);
      asm volatile("" : "+v"(f[db][0]), "+v"(f[db][1]), "+v"(f[db][2]), "+v"(f[db][3]));
    }
    const float p0 = prob(Ea, lg0), p1 = prob(Eb, lg1);
    srun += p0 + p1;
    u32x2 pa = {cvtpk(p0, p1), 0u};
    asm volatile("" : "+v"(pa[0]), "+v"(srun));
    const int vo2 = voff(Ec);
    int vn[3];
    if constexpr (LAST) { vn[0] = voff(En0); vn[1] = voff(En1); vn[2] = voff(El); }
    gate(NS + j + 2);
    issue(vrs, xa, vo2, so2);
    if constexpr (LAST) __builtin_amdgcn_sched_barrier(0); else stamp();
    z0 = vzs[Ec & 0x7fff];
    unsigned bfr[3][4];
#pragma unroll
    for (int db = 0; db < 3; ++db) {
      float y[4];
      vdeq(xb[db], z1, y);
#pragma unroll
      for (int c = 0; c < 4; ++c) bfr[db][c] = cvtpk(f[db][c], y[c]);
      if (!LAST && db == 0) { asm volatile("" : "+v"(bfr[0][0]), "+v"(bfr[0][1]), "+v"(bfr[0][2]), "+v"(bfr[0][3])); }
    }
#pragma unroll
    for (int db = 0; db < 3; ++db)
#pragma unroll
      for (int c = 0; c < 4; ++c)
        oacc[db][c] = __builtin_amdgcn_mfma_f32_16x16x16bf16_1k(__builtin_bit_cast(bf16x4, pa), __builtin_bit_cast(bf16x4, u32x2{bfr[db][c], 0u}), oacc[db][c], 0, 0, 0);
    Ea = Ec; Eb = En0; Ec = En1;
    if constexpr (!LAST) vo1 = voff(Eb);
    else {
      const int tr = go + grel - GLATE;
      wait_t(tr + SD);
      issue(vrs, xc, vn[0], so2);
      wait_t(tr + 2 * SD);
      issue(vrs, xd, vn[1], so2);
      wait_t(tr + 3 * SD);
      issue(vrs, xb, vn[2], so2);
      __builtin_amdgcn_sched_barrier(0);
    }
  };
#pragma unroll 1
  for (int j = 1; j < NS - 6; j += 2) {
    vpair(IC<0>{}, j);
    lds_add(pprog, 1);
    const unsigned d0 = j + 2 - (s0 | 1), d1 = j + 2 - (s1 | 1);
    if (d0 <= 4u || d1 <= 4u) vend(d0 > 4u, ((d0 > 4u && cp == 2 ? 0x0c3 : 0xc21) >> (2 * min(d0, d1))) & 15);
    if (j == (ps < 3 ? 42 * ps - 13 : 0)) adopt();
    if (j == (cp == 2 ? 99 : 71)) {
      for (int spin = 0; spin < (1 << 22) && lds_ld(mrg) < 1; ++spin) __builtin_amdgcn_s_sleep(1);
      asm volatile("" ::: "memory");

      if (mseg == 3) {
        mrun = -INFINITY;
#pragma unroll
        for (int ww = 0; ww < NWAVE; ++ww) mrun = fmaxf(mrun, stats[(cp * NWAVE + ww) * 8 + (m & 3)]);
      }
    }
  }
  static_assert(RS == 6, "");
  vend(2, 15);
  mb = frow(cp) * GRP; msz = mseg == 3 ? NROW * GRP : 0;
  vpair(IC<1>{}, NS - 6);
  float* oslot = reinterpret_cast<float*>(slot1 + w * OSL);
  u32x2 pa;
  unsigned z1, z2, z3;
  {
    const int I = Ia;
    z1 = vzs[Eb & 0x7fff]; z2 = vzs[Ec & 0x7fff]; z3 = vzs[El & 0x7fff];
    const unsigned lg0 = mypl[I * 16], lg1 = mypl[I * 16 + 16], lg2 = mypl[I * 16 + 32], lg3 = mypl[I * 16 + 48];
    const float p0 = prob(Ea, lg0), p1 = prob(Eb, lg1), p2 = prob(Ec, lg2), p3 = prob(El, lg3);
    srun += p0 + p1;
    srun += p2 + p3;
    pa = u32x2{cvtpk(p0, p1), cvtpk(p2, p3)};
    const float sr = rows<false>(srun);
    if (lane >= 60) stats[(3 * NWAVE + w) * 8 + 4 + (m & 3)] = sr;
    lds_add(cnt + 3, 0x100 << w);
    for (int spin = 0; spin < (1 << 22) && lds_ld(mrg) < 2; ++spin) __builtin_amdgcn_s_sleep(1);
    asm volatile("" : "+v"(pa), "+v"(z1), "+v"(z2), "+v"(z3) :: "memory");
  }
  __builtin_amdgcn_sched_barrier(0);
  unsigned b01[3][4];
  float fd[3][4];
#pragma unroll
  for (int db = 0; db < 3; ++db) vdeq(xa[db], z0, fd[db]);
#pragma unroll
  for (int db = 0; db < 3; ++db) {
    float y[4];
    vdeq(xc[db], z1, y);
#pragma unroll
    for (int c = 0; c < 4; ++c) b01[db][c] = cvtpk(fd[db][c], y[c]);
  }
#pragma unroll
  for (int db = 0; db < 3; ++db) {
    vdeq(xd[db], z2, fd[db]);
    asm volatile("" : "+v"(fd[db][0]), "+v"(fd[db][1]), "+v"(fd[db][2]), "+v"(fd[db][3]), "+v"(b01[db][0]), "+v"(b01[db][1]), "+v"(b01[db][2]), "+v"(b01[db][3]));
  }
  __builtin_amdgcn_sched_barrier(0);
#pragma unroll
  for (int db = 0; db < 3; ++db) {
    float y[4];
    vdeq(xb[db], z3, y);
#pragma unroll
    for (int c = 0; c < 4; ++c)
      oacc[db][c] = __builtin_amdgcn_mfma_f32_16x16x16bf16_1k(__builtin_bit_cast(bf16x4, pa), __builtin_bit_cast(bf16x4, u32x2{b01[db][c], cvtpk(fd[db][c], y[c])}), oacc[db][c], 0, 0, 0);
    if (g == 3) {
#pragma unroll
      for (int r = 0; r < 4; ++r)
        *reinterpret_cast<f32x4*>(oslot + r * DH + 64 * db + 4 * m) = f32x4{oacc[db][0][r], oacc[db][1][r], oacc[db][2][r], oacc[db][3][r]};
    }
    __builtin_amdgcn_sched_barrier(0);
  }
  lds_add(cnt + 3, 1 << w);
}

}

extern "C" void kernel_launch(void* const* d_in, const int* in_sizes, int n_in,
                              void* d_out, int out_size, void* d_ws, size_t ws_size,
                              hipStream_t stream) {
  const __hip_bfloat16* q  = (const __hip_bfloat16*)d_in[0];
  const int* qk            = (const int*)d_in[1];
  const int* qv            = (const int*)d_in[2];
  const __hip_bfloat16* ks = (const __hip_bfloat16*)d_in[3];
  const __hip_bfloat16* kz = (const __hip_bfloat16*)d_in[4];
  const __hip_bfloat16* vs = (const __hip_bfloat16*)d_in[5];
  const __hip_bfloat16* vz = (const __hip_bfloat16*)d_in[6];
  kivi_attn_kernel<<<NWG, NTHR + 64, 0, stream>>>(q, qk, qv, ks, kz, vs, vz, (__hip_bfloat16*)d_out, (unsigned char*)d_ws);
}
```

```cpp
#include <hip/hip_runtime.h>
#include <hip/hip_bf16.h>
#include <stdint.h>

namespace {

constexpr int B_ = 8, H_ = 32, SQ = 4, SKV = 4096, DH = 192, GRP = 64, NGK = SKV / GRP;
constexpr int NWG = B_ * H_;
constexpr int NWAVE = 8, NTHR = NWAVE * 64;
constexpr int NST = 43;
constexpr int NQ = NWAVE * NST;
constexpr int NS = 3 * NST;
constexpr int NPR = (NS + 1) / 2;
constexpr int NROW = 22;
constexpr unsigned TBYTES = (unsigned)NWG * SKV * DH * 4u;
constexpr int RS = 6;
#define SLIP 0
#define SLM 5
#define TAU 783
#define ORG0 285
#define GLATE 14
#define PW 2
#define XT0 250
#define HP 1
#define QI 2
#define PWV 1
#define LD 18
#define NSP 3
#define XFL 1
#define FORCE 0
constexpr int XROW = NROW - 1;
constexpr int MROW = 3 * NROW + XROW;
#define SD 20
#define NOH 0
#define HST(i)
constexpr int PLANE = 544;
#define NPUB 32
#define CK0 10
#define PCK 16
#define PDLY 2
#define KP 8
#define DZ 5
#define DZU 0
#define RDN 8
#define RUP 20
#define JMP 100
constexpr int WS_PART = 0;
constexpr int WS_PACE = 2 << 20;
constexpr int WS_X = 4 << 20;
constexpr int WS_BYTES = WS_X + 11 * 131072;
constexpr int AUX_NT = 2, AUX_SC1 = 16;
constexpr int OSL = SQ * DH * 4;

using u32x4  = __attribute__((ext_vector_type(4))) unsigned;
using u32x8  = __attribute__((ext_vector_type(8))) unsigned;
using u32x2  = __attribute__((ext_vector_type(2))) unsigned;
using i32x4  = __attribute__((ext_vector_type(4))) int;
using f32x4  = __attribute__((ext_vector_type(4))) float;
using bf16x8 = __attribute__((ext_vector_type(8))) short;
using bf16x4 = __attribute__((ext_vector_type(4))) short;

using f32x2  = __attribute__((ext_vector_type(2))) float;
using bfx2   = __attribute__((ext_vector_type(2))) __bf16;
__device__ __forceinline__ unsigned cvtpk(float lo, float hi) {
  f32x2 f = {lo, hi};
  return __builtin_bit_cast(unsigned, __builtin_convertvector(f, bfx2));
}
__device__ __forceinline__ float bflo(unsigned p) { return __uint_as_float(p << 16); }
__device__ __forceinline__ float bfhi(unsigned p) { return __uint_as_float(p & 0xffff0000u); }
__device__ __forceinline__ float bfround(float x) { return bflo(cvtpk(x, x)); }

__device__ __forceinline__ __amdgpu_buffer_rsrc_t rsrc(const void* p, unsigned bytes) { return __builtin_amdgcn_make_buffer_rsrc((void*)p, 0, (int)bytes, 0x00020000); }
__device__ __forceinline__ u32x4 ldnt4(__amdgpu_buffer_rsrc_t r, int voff) { return __builtin_amdgcn_raw_buffer_load_b128(r, voff, 0, AUX_NT); }
__device__ __forceinline__ unsigned div3(unsigned x) { return ((x & 0xffffu) * 43691u) >> 17; }

__device__ __forceinline__ i32x4 ldbuf(__amdgpu_buffer_rsrc_t r, int voff, unsigned soff) {
  return __builtin_bit_cast(i32x4, __builtin_amdgcn_raw_buffer_load_b128(r, voff, (int)soff, AUX_NT));
}

__device__ __forceinline__ unsigned deq2(int c0, int c1, unsigned z, unsigned s) {
  unsigned t = cvtpk((float)c0 - bflo(z), (float)c1 - bfhi(z));
  return cvtpk(bflo(t) * bflo(s), bfhi(t) * bfhi(s));
}

__device__ __forceinline__ unsigned lds_addr(int* p) { return (unsigned)(size_t)(__attribute__((address_space(3))) int*)p; }
__device__ __forceinline__ void lds_add(int* p, int v) {
  asm volatile("s_mov_b64 exec, 1\n\tds_add_u32 %0, %1\n\ts_mov_b64 exec, -1" :: "v"(lds_addr(p)), "v"(v) : "memory");
}
__device__ __forceinline__ void lds_max(int* p, int v) {
  asm volatile("s_mov_b64 exec, 1\n\tds_max_i32 %0, %1\n\ts_mov_b64 exec, -1" :: "v"(lds_addr(p)), "v"(v) : "memory");
}

template <bool MAX> __device__ __forceinline__ float rows(float x) {
  const auto a = __builtin_amdgcn_permlane16_swap(__float_as_uint(x), __float_as_uint(x), false, false);
  x = MAX ? fmaxf(__uint_as_float(a[0]), __uint_as_float(a[1])) : __uint_as_float(a[0]) + __uint_as_float(a[1]);
  const auto b = __builtin_amdgcn_permlane32_swap(__float_as_uint(x), __float_as_uint(x), false, false);
  return MAX ? fmaxf(__uint_as_float(b[0]), __uint_as_float(b[1])) : __uint_as_float(b[0]) + __uint_as_float(b[1]);
}

template <int N> __device__ __forceinline__ void wait_tv(unsigned tg, int d) {
  asm volatile("s_cmp_lt_i32 %0, 1\n\ts_cbranch_scc1 2f\n1:\n\ts_memrealtime vcc\n\ts_waitcnt lgkmcnt(0)\n\ts_sub_i32 %0, %1, vcc_lo\n\t"
               "s_cmp_gt_i32 %0, 0\n\ts_cbranch_scc1 1b\n2:\n\ts_waitcnt vmcnt(%2)" : "+s"(d) : "s"(tg), "n"(N) : "memory", "scc", "vcc");
}

__device__ __forceinline__ void lds_barrier() { asm volatile("s_waitcnt lgkmcnt(0)\n\ts_barrier" ::: "memory"); }

__device__ __forceinline__ int lds_ld(const int* p) {
  return __builtin_amdgcn_readfirstlane(__hip_atomic_load(p, __ATOMIC_RELAXED, __HIP_MEMORY_SCOPE_WORKGROUP));
}

__device__ __forceinline__ int load_time(const void* p, unsigned o) {
  unsigned long long t0, t1; int x;
  asm volatile("s_memtime %0\n\ts_load_dword %2, %3, %4\n\ts_waitcnt lgkmcnt(0)\n\ts_memtime %1\n\ts_waitcnt lgkmcnt(0)"
               : "=&s"(t0), "=&s"(t1), "=&s"(x) : "s"(p), "s"(o) : "memory");
  return (int)(t1 - t0);
}

__device__ __forceinline__ unsigned pick(bool c, unsigned a, unsigned b) { return c ? a : b; }

#define MAP 1
__device__ __forceinline__ int wgmap(int b) {
  if (MAP == 1) return (b & ~15) | ((b & 3) << 2) | ((b >> 2) & 3);
  if (MAP == 2) return (b & 0xb0) | ((b & 1) << 2) | ((b & 2) << 5) | ((b >> 2) & 3) | ((b >> 3) & 8);
  if (MAP == 3) return (b & 0x70) | ((b & 1) << 2) | ((b & 2) << 6) | ((b >> 2) & 3) | ((b >> 4) & 8);
  return (b & ~7) | ((b & 3) << 1) | ((b >> 2) & 1);
}

template <int N> struct IC { static constexpr int value = N; };

__global__ __launch_bounds__(NTHR + 64, 3)
void kivi_attn_kernel(const __hip_bfloat16* __restrict__ q_, const int* __restrict__ qk, const int* __restrict__ qv,
                      const __hip_bfloat16* __restrict__ ks_, const __hip_bfloat16* __restrict__ kz_,
                      const __hip_bfloat16* __restrict__ vs_, const __hip_bfloat16* __restrict__ vz_,
                      __hip_bfloat16* __restrict__ out, unsigned char* ws) {
  __shared__ __attribute__((aligned(16))) unsigned char kst[NWAVE * OSL];
  __shared__ __attribute__((aligned(16))) unsigned short pl[NWAVE * NS * 16];
  __shared__ __attribute__((aligned(16))) unsigned kzs[MROW * DH];
  __shared__ __attribute__((aligned(16))) unsigned vzs[MROW * GRP];
  __shared__ __attribute__((aligned(16))) unsigned short tab[(NS + NST) * NWAVE * 4];
  __shared__ __attribute__((aligned(16))) unsigned short qs[3 * SQ * DH];
  __shared__ __attribute__((aligned(16))) float stats[4 * NWAVE * 8];
  __shared__ __attribute__((aligned(16))) float xmax[NWAVE * 4];
  __shared__ int cnt[4], rdy[1], pprog[1], mrg[1], xfl[1];
  __shared__ __attribute__((aligned(8))) int org[4];

  const int wg = wgmap(blockIdx.x);
  const int tid = threadIdx.x, lane = tid & 63;
  u32x8 kp; u32x2 tw2;
  asm volatile("s_load_dwordx8 %0, %2, 0x18\n\ts_memrealtime %1" : "=&s"(kp), "=&s"(tw2) : "s"(__builtin_amdgcn_kernarg_segment_ptr()));
  const unsigned char* ca; unsigned cn;
  asm volatile("s_getpc_b64 %0\n.Lkivi_code_a:\n\ts_mov_b32 %1, .Lkivi_code_b-.Lkivi_code_a" : "=s"(ca), "=s"(cn));
  const unsigned cw = __builtin_amdgcn_raw_buffer_load_b32(rsrc(ca, cn), tid * 128, 0, 0);
  const int w = __builtin_amdgcn_readfirstlane(tid >> 6);
  const int m = lane & 15, g = lane >> 4;

  const int ps = (0x2ff37 >> (2 * ((wg >> 1) - 9 * (((wg >> 1) * 57) >> 9)))) & 3;
  const int l0 = ps == 0 ? LD : 0;
  const int r2 = 2 * NROW + (ps < 2 ? XROW : 0);
  auto head = [=](int p) { return (int)div3(p * NWG + wg); };
  auto role = [=](int p) { return p * NWG + wg - 3 * head(p); };
  auto frow = [=](int p) { return (int)pick(p == 2, r2, (p == 0) == (ps == 0) ? NROW : 0); };
  auto lrow = [=](int p) { return frow(p) - (p == ps ? 0 : role(p) * (NROW - 1)); };
  auto gbase = [=](int p) { return head(p) * NGK - lrow(p); };
  const int pw = pick(w < 4, (w >> 1) ^ (ps == 0), 2);
  unsigned kso = (head(pw) * NGK + pick(pw == ps, 0, role(pw) * (NROW - 1))) * (DH * 2) + (w & 1) * 4096;
  asm volatile("s_waitcnt lgkmcnt(0)" : "+s"(kp), "+s"(tw2), "+s"(kso));
  const unsigned tw = tw2[0];
  auto kptr = [=](int i) { return reinterpret_cast<const void*>(kp[2 * i] | ((unsigned long long)kp[2 * i + 1] << 32)); };
  const __amdgpu_buffer_rsrc_t ksr = rsrc(kptr(0), NWG * NGK * DH * 2), kzr = rsrc(kptr(1), NWG * NGK * DH * 2), vsr = rsrc(kptr(2), NWG * SKV * 2), vzr = rsrc(kptr(3), NWG * SKV * 2);
  if (tid == NTHR - 64) {
    rdy[0] = 0; pprog[0] = 0; cnt[0] = 0; cnt[1] = 0; cnt[2] = 0; cnt[3] = 0; mrg[0] = 0; xfl[0] = 0;
    org[0] = ORG0 << 8; org[1] = 1; org[2] = (int)tw;
  }
  u32x4 sz[4], ss[4];
  auto kload = [&]() {
#pragma unroll
    for (int i = 0; i < 4; ++i) {
      sz[i] = __builtin_amdgcn_raw_buffer_load_b128(kzr, 16 * lane + 1024 * i, kso, AUX_NT);
      ss[i] = __builtin_amdgcn_raw_buffer_load_b128(ksr, 16 * lane + 1024 * i, kso, AUX_NT);
    }
  };
  if (w < 4) kload();
  else {
#pragma unroll
    for (int i = 0; i < 4; ++i) asm volatile("" : "=v"(sz[i]), "=v"(ss[i]));
  }
  lds_barrier();
  const int gba = gbase(ps == 0), gbb = gbase(ps != 0), gb2 = gbase(2);
  auto sc_off = [=](int v, int RL) { return 16 * v + 16 * RL * gba + (v >= NROW * RL ? 16 * RL * (gbb - gba) : 0) + (v >= r2 * RL ? 16 * RL * (gb2 - gbb) : 0); };
  const int cp = 2 - role(0);
  const int ord = cp < 2 ? 0x24 : (ps == 1 ? 0x09 : 0x18);
  const __amdgpu_buffer_rsrc_t krs = rsrc(qk, TBYTES), vrs = rsrc(qv, TBYTES);
  auto put_k = [&](int v, const u32x4& z, const u32x4& s) {
    *reinterpret_cast<u32x4*>(kzs + 8 * v) = u32x4{z[0], z[1], s[0], s[1]};
    *reinterpret_cast<u32x4*>(kzs + 8 * v + 4) = u32x4{z[2], z[3], s[2], s[3]};
  };
  auto put_v = [&](int v, const u32x4& z, const u32x4& s) {
#pragma unroll
    for (int h = 0; h < 2; ++h)
      *reinterpret_cast<u32x4*>(vzs + 8 * v + 4 * h) = u32x4{(z[2 * h] & 0xffffu) | (s[2 * h] << 16), (z[2 * h] >> 16) | (s[2 * h] & 0xffff0000u),
                                                             (z[2 * h + 1] & 0xffffu) | (s[2 * h + 1] << 16), (z[2 * h + 1] >> 16) | (s[2 * h + 1] & 0xffff0000u)};
  };
  unsigned char* slot1 = reinterpret_cast<unsigned char*>(kzs) + 3 * 4096;

  if (w == NWAVE) {
    if (HP) __builtin_amdgcn_s_setprio(3);
    const __amdgpu_buffer_rsrc_t wrs = rsrc(ws, WS_BYTES);
    const int mem = wg & 1, xh = head(min(ps, 2)), xb = l0 + ps * NST;
    auto xbase = [=](int who) { const int y = (int)div3(xh) + who; return WS_X + (y >> 3) * 131072 + (y & 7) * 4096; };
    int xs = 4, par[2] = {0, 0};
    const unsigned rnd = (tw >> 12) * 0x9E3779B1u, k8 = lane & 7;
    const unsigned line = (unsigned)xh * (SKV * DH * 4u) + ((5u * k8 + (rnd >> 26)) & 63u) * 32768u + (((k8 + 1) & 2) << 12) + ((k8 & 2) << 11) + ((k8 & 4) << 12) + (((rnd >> 21) + 11u * k8) & 31u) * 128u;
    unsigned pre0 = 0, pre1 = 0;
    if (ps < 3 && mem && lane < 2 * NSP) { pre0 = __builtin_amdgcn_raw_buffer_load_b32(krs, line, 0, 0); pre1 = __builtin_amdgcn_raw_buffer_load_b32(vrs, line, 0, 0); }
    __builtin_amdgcn_sched_barrier(0);
    u32x4 ql[6];
    const int l48 = min(lane, 47), vk = (l48 < 32 ? (l48 >> 4) * NROW : r2) * 24 + 512 + m;
#pragma unroll
    for (int i = 0; i < 6; ++i) ql[i] = ldnt4(rsrc(q_, NWG * SQ * DH * 2), head(i >> 1) * (SQ * DH * 2) + 16 * (l48 + 48 * (i & 1)));
    const u32x2 ep = __builtin_amdgcn_raw_buffer_load_b64(wrs, WS_PACE + (((wg + 2 - role(min(lane, 2))) & 255) * PLANE + 8) * 8, 0, AUX_SC1);
    const int ov = ps < 3 || lane < 16 ? sc_off(512 + lane, 8) : -1;
    const u32x4 kz = ldnt4(kzr, sc_off(vk, 24)), ks = ldnt4(ksr, sc_off(vk, 24)), vz = ldnt4(vzr, ov), vs = ldnt4(vsr, ov);
    __builtin_amdgcn_sched_barrier(0);
    const unsigned t0 = (unsigned)lds_ld(org + 2);
    asm volatile("" :: "v"(cw), "v"(pre0), "v"(pre1));
#pragma unroll
    for (int i = 0; i < 6; ++i) *reinterpret_cast<u32x4*>(qs + 8 * (l48 + 48 * i)) = ql[i];
    asm volatile("s_waitcnt lgkmcnt(0)" ::: "memory");
    if (lane == 0) __hip_atomic_fetch_add(rdy, 4, __ATOMIC_RELAXED, __HIP_MEMORY_SCOPE_WORKGROUP);
    __builtin_amdgcn_sched_barrier(0);
    put_k(vk, kz, ks);
    put_v(512 + lane, vz, vs);
    const unsigned long long tag = (ep[0] | ((unsigned long long)ep[1] << 32)) * 0x9E3779B97F4A7C15ull + 0xD1B54A32D192ED03ull;
    const unsigned xlo = __builtin_amdgcn_readlane((unsigned)tag, min(ps, 2)) ^ 0x5bd1e995u, xhi = __builtin_amdgcn_readlane((unsigned)(tag >> 32), min(ps, 2));
    auto xput = [&](int i, int v) { if (lane < 16) __builtin_amdgcn_raw_buffer_store_b64(u32x2{xlo + v, xhi}, wrs, xbase(mem) + 3840 + 128 * i + 8 * lane, 0, AUX_SC1); };
    auto xget = [&](int i) {
      asm volatile("" ::: "memory");
      const u32x2 x = __builtin_amdgcn_raw_buffer_load_b64(wrs, xbase(mem ^ 1) + 3840 + 128 * i, 0, AUX_SC1);
      asm volatile("" ::: "memory");
      const unsigned d = __builtin_amdgcn_readfirstlane(x[0]) - xlo;
      return __builtin_amdgcn_readfirstlane(x[1]) == xhi && d <= 4u ? (int)d : 0;
    };
    auto flip = [&]() {
#pragma unroll 1
      for (int v = lane; v < NST * 4; v += 64) {
        u32x4* a = reinterpret_cast<u32x4*>(tab + xb * 32) + v; u32x4* b = reinterpret_cast<u32x4*>(tab + NS * 32) + v;
        const u32x4 x = *a, y = *b;
        *a = y; *b = x;
      }
    };
    {
      if (ps < 3) {
        int x = 0, mn[2][2] = {{1 << 20, 1 << 20}, {1 << 20, 1 << 20}};
        while ((int)((unsigned)__builtin_amdgcn_s_memrealtime() - t0) < XT0) __builtin_amdgcn_s_sleep(4);
#pragma unroll 1
        for (int k = -1; k < 2 * NSP; ++k) {
          const bool kk = k < 3;
          const int va = (kk ? (frow(ps) + NROW) * 24 + 128 * (k + 1) : 576) + lane, vc = min(va + 64, (kk ? (frow(ps) + NROW + XROW) * 24 : MROW * 8) - 1);
          u32x4 z[2], sc[2];
          if (k < 4) {
#pragma unroll
            for (int i = 0; i < 2; ++i) { const int o = sc_off(i ? vc : va, kk ? 24 : 8); z[i] = ldnt4(kk ? kzr : vzr, o); sc[i] = ldnt4(kk ? ksr : vsr, o); }
          } else asm volatile("" : "=v"(z[0]), "=v"(z[1]), "=v"(sc[0]), "=v"(sc[1]));
          __builtin_amdgcn_sched_barrier(0);
          if (!mem) {
#pragma unroll
            for (int i = 0; i < 2; ++i) {
              int d = load_time(i ? qv : qk, __builtin_amdgcn_readlane(line, k & 7));
              if (k < 0 || d < 270) d = 1 << 20;
              if ((k + 1) & 2) mn[i][1] = min(mn[i][1], d); else mn[i][0] = min(mn[i][0], d);
            }
          }
          if (kk) { put_k(va, z[0], sc[0]); put_k(vc, z[1], sc[1]); }
          else if (k == 3) { put_v(va, z[0], sc[0]); put_v(vc, z[1], sc[1]); }
          if (lds_ld(pprog) >= 8 && lane == 0) org[1] = 0;
        }
        if (!mem) {
          x = FORCE ? FORCE & 3 : (mn[0][1] < mn[0][0]) + 2 * (mn[1][1] < mn[1][0]);
          xput(0, 4 - x);
        } else {
          for (int sp = 0; sp < (1 << 20) && !(x = xget(0)); ++sp) if (lds_ld(pprog) >= 8 && lane == 0) org[1] = 0;
          --x;
        }
        par[0] = x & 1; par[1] = x >> 1;
        for (int sp = 0; sp < (1 << 22) && ((lds_ld(rdy) >> 8) & 255) < NWAVE; ++sp) __builtin_amdgcn_s_sleep(1);
        asm volatile("" ::: "memory");
        if (par[0]) flip();
        if (par[0] != par[1]) xs = 1;
        else if (lane == 0) __hip_atomic_store(xfl, 1, __ATOMIC_RELAXED, __HIP_MEMORY_SCOPE_WORKGROUP);
      }
      asm volatile("s_waitcnt lgkmcnt(0)" ::: "memory");
      if (lane == 0) __hip_atomic_fetch_add(rdy, 0x100, __ATOMIC_RELAXED, __HIP_MEMORY_SCOPE_WORKGROUP);
      if (HP) __builtin_amdgcn_s_setprio(0);
    }
    auto pslot = [](int y) { return WS_PART + (y >> 5) * 131072 + (y & 31) * 1024; };
    const int pln = g * 32768 + g * 256 + 16 * m;
    auto part = [&](int t) { const int p = (t + 1 + cp) % 3; return pslot(2 * head(p) + (p == ps ? par[1] : t)); };
    int vp0 = part(0) + pln, vp1 = part(1) + pln, vf = (lane & 16 ? part(1) : part(0)) + 768 + 8 * m;
    int lz = (lane & 16) * 256 + SQ * DH * 4 + 8 * (lane & 3);
    const unsigned long long tg = __shfl(tag, (g + 1 + cp) % 3);
    u32x2 tgv = {(unsigned)tg, (unsigned)(tg >> 32)};
    const int ch = head(cp);
    unsigned ctlo = __builtin_amdgcn_readlane((unsigned)tag, cp), cthi = __builtin_amdgcn_readlane((unsigned)(tag >> 32), cp);
    int so = pslot(2 * ch);
    int fw = so + (lane & 1) * 1024 + 896, eo = WS_PACE + wg * (PLANE * 8) + 64, oo = ch * (SQ * DH * 2) + 8 * lane;
    asm volatile("" : "+v"(vp0), "+v"(vp1), "+v"(vf), "+v"(lz), "+v"(tgv), "+v"(fw), "+v"(eo), "+v"(oo));
    asm volatile("" : "+s"(ctlo), "+s"(cthi), "+s"(so));
    auto xvec = [=](int v, const float* mx) {
      const int ww = v / 86;
      return v < 688 ? reinterpret_cast<unsigned char*>(pl) + ww * (NS * 32) + xb * 32 + 16 * (v - 86 * ww) : (unsigned char*)mx;
    };
    unsigned xt = 0;
    auto xstep = [&]() {
      const unsigned t = (unsigned)__builtin_amdgcn_s_memrealtime();
      u32x4 y[11];
      if (xs == 1) {
        if (((lds_ld(rdy) >> (ps < 2 ? 24 : 16)) & 255) < NWAVE) return;
        asm volatile("" ::: "memory");
#pragma unroll
        for (int i = 0; i < 11; ++i) { y[i] = *reinterpret_cast<const u32x4*>(xvec(lane + 64 * i, stats + (ps * NWAVE + (lane & 7)) * 8)); asm volatile("" : "+v"(y[i])); }
        if (XFL && lane == 0) org[1] = 1;
#pragma unroll
        for (int i = 0; i < 11; ++i) __builtin_amdgcn_raw_buffer_store_b128(y[i], wrs, (xbase(mem) + pln) ^ (i << 8), 0, AUX_SC1 | AUX_NT);
        asm volatile("s_waitcnt vmcnt(0)" ::: "memory");
        xput(1, 1);
        xs = 2; xt = t + 300;
        flip();
      } else if ((int)(t - xt) > 0) {
        xt = t + 100;
        if (!xget(1)) return;
#pragma unroll
        for (int i = 0; i < 11; ++i) y[i] = __builtin_amdgcn_raw_buffer_load_b128(wrs, (xbase(mem ^ 1) + pln) ^ (i << 8), 0, AUX_SC1 | AUX_NT);
#pragma unroll
        for (int i = 0; i < 11; ++i) *reinterpret_cast<u32x4*>(xvec(lane + 64 * i, xmax + 4 * (lane & 7))) = y[i];
        xs = 4;
        asm volatile("s_waitcnt lgkmcnt(0)" ::: "memory");
        if (lane == 0) __hip_atomic_store(xfl, 3, __ATOMIC_RELAXED, __HIP_MEMORY_SCOPE_WORKGROUP);
        if (XFL && lane == 0) org[1] = 0;
      }
    };
    int last = 0, last8 = 0, nck = CK0, want = 0, ref = 0, o1 = 1 << 20, o2 = 1 << 20, jmp = 0;
    bool on = false;
    float* park = reinterpret_cast<float*>(kzs);
    int qv3[3];
#pragma unroll
    for (int u = 0; u < 3; ++u) qv3[u] = (lane + 64 * u) / 48;
    constexpr int TOTAL = 2 * NPR;
    int nred = 0;
    for (int spin = 0; nred < 3 && spin < (1 << 23); ++spin) {
      const int n8 = lds_ld(pprog), n = n8 >> 3;
      if (n8 != last8 && n < TOTAL - 4) {
        if (on) {
          const int d = ref - lds_ld(org) / 256, rate = min(max((d < 0 ? min(d + DZ, 0) : max(d - DZU, 0)) * 32 / KP, -RDN * 32), RUP * 32);
          if (lane == 0) {
            if (rate) __hip_atomic_fetch_add(org, min(n8 - last8, 8) * rate, __ATOMIC_RELAXED, __HIP_MEMORY_SCOPE_WORKGROUP);
            org[1] = rate < -64 || (XFL && xs == 2);
          }
        }
        last8 = n8;
      }
      if (last < 1 && n >= 1 && lane == 0) org[1] = 0;
      if (xs < 4) xstep();
      if (n != last && n < TOTAL - 4) {
        const int o = lds_ld(org) >> 8;
        if (o - o2 > JMP) { ref += o - o2; jmp += o - o2; o1 = o; }
        o2 = o1; o1 = o;
        if (n >= nck) {
          if (lane == 0 && (blockIdx.x >> 3) % (NWG / NPUB) == 0) __builtin_amdgcn_raw_buffer_store_b64(u32x2{t0 + (unsigned)o, (unsigned)nck}, wrs, eo - 64, 0, AUX_SC1);
          want = nck; nck += PCK; jmp = 0;
        }
        if (want && n >= want + PDLY) {
          asm volatile("" ::: "memory");
          const u32x2 x = __builtin_amdgcn_raw_buffer_load_b64(wrs, WS_PACE + wgmap(((lane % NPUB) >> 3) * (8 * NWG / NPUB) + (lane & 7)) * (PLANE * 8), 0, AUX_SC1);
          const int r = (int)(x[0] - t0) - o;
          const bool ok = (int)x[1] == want && r > -2000 && r < 2000;
          float e = ok ? (float)r : 0.f;
          e += __shfl_xor(e, 1); e += __shfl_xor(e, 2); e += __shfl_xor(e, 4); e += __shfl_xor(e, 8);
          e = rows<false>(e);
          const int c = __popcll(__ballot(ok));
          if (c > 32) { ref = o + __builtin_amdgcn_readfirstlane((int)(e / c)) + jmp; on = true; }
          want = 0;
        }
        last = n;
      }
      if ((lds_ld(cnt + nred) | (nred == 2 ? 254 : 0)) == 255) {
        for (int sp = 0; sp < (1 << 22) && ((lds_ld(rdy) >> 16) & 255) < NWAVE; ++sp) __builtin_amdgcn_s_sleep(1);
        asm volatile("" ::: "memory");
        const int pm = (ord >> (2 * nred)) & 3;
        const int t = (pm + 2 - cp) % 3;
        const float* st = stats + pm * NWAVE * 8;
        const unsigned char* sl = (nred & 1 ? slot1 : kst) + 16 * lane;
        f32x4 o[3]; float mx[3], sm[3], f[3][NWAVE];
#pragma unroll
        for (int u = 0; u < 3; ++u) {
          mx[u] = -INFINITY;
#pragma unroll
          for (int ww = 0; ww < NWAVE; ++ww) { f[u][ww] = st[ww * 8 + qv3[u]]; mx[u] = fmaxf(mx[u], f[u][ww]); }
#pragma unroll
          for (int ww = 0; ww < NWAVE; ++ww) { f[u][ww] = __expf(f[u][ww] - mx[u]); asm volatile("" : "+v"(f[u][ww])); }
          o[u] = f32x4{0.f, 0.f, 0.f, 0.f}; sm[u] = 0.f;
        }
#pragma unroll
        for (int w2 = 0; w2 < NWAVE; w2 += 2) {
          f32x4 y[2][3]; float z[2][3];
          for (int sp = 0; sp < (1 << 22); ++sp) {
            const int c = __hip_atomic_load(cnt + nred, __ATOMIC_RELAXED, __HIP_MEMORY_SCOPE_WORKGROUP);
            asm volatile("" ::: "memory");
#pragma unroll
            for (int k = 0; k < 2; ++k)
#pragma unroll
              for (int u = 0; u < 3; ++u) {
                y[k][u] = *reinterpret_cast<const f32x4*>(sl + (w2 + k) * OSL + 1024 * u);
                z[k][u] = st[(w2 + k) * 8 + 4 + qv3[u]];
              }
            asm volatile("" ::: "memory");
            if (((__builtin_amdgcn_readfirstlane(c) >> w2) & 3) == 3) break;
          }
#pragma unroll
          for (int k = 0; k < 2; ++k)
#pragma unroll
            for (int u = 0; u < 3; ++u) { o[u] += f[u][w2 + k] * y[k][u]; sm[u] += f[u][w2 + k] * z[k][u]; }
          asm volatile("" : "+v"(o[0]), "+v"(o[1]), "+v"(o[2]), "+v"(sm[0]), "+v"(sm[1]), "+v"(sm[2]));
        }
#pragma unroll
        for (int u = 0; u < 3; ++u) {
          *reinterpret_cast<f32x4*>(park + t * 1024 + 4 * (lane + 64 * u)) = o[u];
          *reinterpret_cast<f32x2*>(park + t * 1024 + SQ * DH + 2 * qv3[u]) = f32x2{mx[u], sm[u]};
        }
        ++nred;
        asm volatile("s_waitcnt lgkmcnt(0)" ::: "memory");
        if (lane == 0) __hip_atomic_store(mrg, nred, __ATOMIC_RELAXED, __HIP_MEMORY_SCOPE_WORKGROUP);
        continue;
      }
      __builtin_amdgcn_s_sleep(4);
    }
    {
      u32x4 y[2][3];
#pragma unroll
      for (int t = 0; t < 2; ++t)
#pragma unroll
        for (int u = 0; u < 3; ++u) y[t][u] = *reinterpret_cast<const u32x4*>(park + t * 1024 + 4 * (lane + 64 * u));
      u32x2 z = *reinterpret_cast<const u32x2*>(reinterpret_cast<const unsigned char*>(park) + lz);
      asm volatile("" : "+v"(z));
      HST(0);
      if (NOH & 1) { vp0 = vp1 = vf = 1 << 30; }
      if (NOH & 2) { fw = 1 << 30; so = 1 << 30; ctlo = cthi = 0; }
#pragma unroll
      for (int t = 0; t < 2; ++t)
#pragma unroll
        for (int u = 0; u < 3; ++u) __builtin_amdgcn_raw_buffer_store_b128(y[t][u], wrs, (t ? vp1 : vp0) ^ (u << 8), 0, AUX_SC1);
      if (lane < 32) __builtin_amdgcn_raw_buffer_store_b64(z, wrs, vf, 0, AUX_SC1);
      asm volatile("s_waitcnt vmcnt(0)" ::: "memory");
      if (lane < 32) __builtin_amdgcn_raw_buffer_store_b64(tgv, wrs, vf + 128, 0, AUX_SC1);
      HST(1);
    }
    {
      f32x4 x[2][3], ro[3]; u32x2 ms[2][3]; float rm[3], rs[3];
#pragma unroll
      for (int u = 0; u < 3; ++u) {
        ro[u] = *reinterpret_cast<const f32x4*>(park + 2 * 1024 + 4 * (lane + 64 * u));
        const f32x2 a = *reinterpret_cast<const f32x2*>(park + 2 * 1024 + SQ * DH + 2 * qv3[u]);
        rm[u] = a[0]; rs[u] = a[1];
      }
      const float* st = stats + 3 * NWAVE * 8;
      auto try3 = [&](int ww) {
        const int c = __hip_atomic_load(cnt + 3, __ATOMIC_RELAXED, __HIP_MEMORY_SCOPE_WORKGROUP);
        asm volatile("" ::: "memory");
        f32x4 y[3];
#pragma unroll
        for (int u = 0; u < 3; ++u) y[u] = *reinterpret_cast<const f32x4*>(slot1 + ww * OSL + 16 * (lane + 64 * u));
        asm volatile("" ::: "memory");
        const int ok = (__builtin_amdgcn_readfirstlane(c) >> ww) & 1;
        if (ok) {
#pragma unroll
          for (int u = 0; u < 3; ++u) ro[u] += y[u];
        }
        return ok;
      };
      constexpr int TB = 4;
      int nw = 0;
#pragma unroll 1
      for (int spin = 0; spin < (1 << 21); ++spin) {
        asm volatile("" ::: "memory");
        const u32x2 fl = __builtin_amdgcn_raw_buffer_load_b64(wrs, fw, 0, AUX_SC1);
        asm volatile("" ::: "memory");
#pragma unroll 1
        for (int k = 0; k < 4 && nw < NWAVE - TB && try3(nw); ++k) ++nw;
        if (__all(fl[0] == ctlo && fl[1] == cthi)) break;
        __builtin_amdgcn_s_sleep(1);
      }
      asm volatile("" ::: "memory");
      HST(2);
#pragma unroll
      for (int t = 0; t < 2; ++t)
#pragma unroll
        for (int u = 0; u < 3; ++u) {
          x[t][u] = __builtin_bit_cast(f32x4, __builtin_amdgcn_raw_buffer_load_b128(wrs, NOH & 2 ? 1 << 30 : pln ^ (u << 8), NOH & 2 ? 0 : so + t * 1024, AUX_SC1));
          ms[t][u] = __builtin_amdgcn_raw_buffer_load_b64(wrs, NOH & 2 ? 1 << 30 : 768 + 8 * qv3[u], NOH & 2 ? 0 : so + t * 1024, AUX_SC1);
        }
      __builtin_amdgcn_sched_barrier(0);
      for (int q = 0; q < (1 << 22) && ((lds_ld(cnt + 3) >> 8) & 255) != 255; ++q) __builtin_amdgcn_s_sleep(1);
      asm volatile("" ::: "memory");
#pragma unroll
      for (int u = 0; u < 3; ++u)
#pragma unroll
        for (int ww = 0; ww < NWAVE; ++ww) rs[u] += st[ww * 8 + 4 + qv3[u]];
      asm volatile("" : "+v"(rs[0]), "+v"(rs[1]), "+v"(rs[2]));
#pragma unroll 1
      for (int spin = 0; spin < (1 << 22) && nw < NWAVE - TB; ++spin) nw += try3(nw);
      __builtin_amdgcn_sched_barrier(0);
      float fc[3];
#pragma unroll
      for (int u = 0; u < 3; ++u) {
        const float m0 = __uint_as_float(ms[0][u][0]), m1 = __uint_as_float(ms[1][u][0]);
        const float mx = fmaxf(rm[u], fmaxf(m0, m1));
        const float f0 = __expf(m0 - mx), f1 = __expf(m1 - mx), f2 = __expf(rm[u] - mx);
        const float r = 1.f / (f0 * __uint_as_float(ms[0][u][1]) + f1 * __uint_as_float(ms[1][u][1]) + f2 * rs[u]);
        fc[u] = f2 * r;
        x[0][u] = (f0 * r) * x[0][u] + (f1 * r) * x[1][u];
        asm volatile("" : "+v"(x[0][u]), "+v"(fc[u]));
      }
      __builtin_amdgcn_sched_barrier(0);
      HST(3);
      const __amdgpu_buffer_rsrc_t ors = rsrc(out, NWG * SQ * DH * 2);
      {
        f32x4 y[TB][3];
        for (int sp = 0; sp < (1 << 22); ++sp) {
          const int c = __hip_atomic_load(cnt + 3, __ATOMIC_RELAXED, __HIP_MEMORY_SCOPE_WORKGROUP);
          asm volatile("" ::: "memory");
#pragma unroll
          for (int u = 0; u < 3; ++u)
#pragma unroll
            for (int k = 0; k < TB; ++k) y[k][u] = *reinterpret_cast<const f32x4*>(slot1 + (NWAVE - TB + k) * OSL + 16 * (lane + 64 * u));
          asm volatile("" ::: "memory");
          if ((__builtin_amdgcn_readfirstlane(c) & 255) == 255) break;
        }
#pragma unroll
        for (int u = 0; u < 3; ++u) {
#pragma unroll
          for (int k = 0; k < TB; ++k) ro[u] += y[k][u];
          const f32x4 o = x[0][u] + fc[u] * ro[u];
          __builtin_amdgcn_raw_buffer_store_b64(u32x2{cvtpk(o[0], o[1]), cvtpk(o[2], o[3])}, ors, oo + 512 * u, 0, AUX_SC1);
        }
        if (lane == 0) __builtin_amdgcn_raw_buffer_store_b64(u32x2{ctlo, cthi}, wrs, eo, 0, AUX_SC1);
      }
    }
    asm volatile(".Lkivi_code_b:");
    return;
  }

  const int vb = (w < 6 ? frow(pw) * 24 : 0) + (w & 1) * 256 + lane;
  if (w >= 6) {
#pragma unroll
    for (int i = 0; i < 4; ++i) { const int o = sc_off(vb + 64 * i, 8); sz[i] = ldnt4(vzr, o); ss[i] = ldnt4(vsr, o); }
  } else if (w >= 4) kload();
  __builtin_amdgcn_sched_barrier(0);
  const float scale = 0.07216878364870322f;
  unsigned char* myst = kst + w * OSL;
  unsigned short* mypl = pl + w * (NS * 16) + 4 * (m & 3) + g;
  const unsigned short* mytab = tab + w * 4 + g;
  const int mseg = m >> 2;
  unsigned mb = frow(mseg) * GRP;
  unsigned msz = mseg == 3 ? 0 : (mseg == ps ? NROW + XROW : NROW) * GRP;
  auto sbase = [=](int p) { return (unsigned)__builtin_amdgcn_readfirstlane(gbase(p) * (GRP * DH * 4)); };
  const unsigned sb0 = sbase(0), sb1 = sbase(1), sb2 = sbase(2);
  auto soff = [=](int i) { unsigned r = pick((unsigned)(i - l0) < (unsigned)NST, sb0, pick(i < 2 * NST, sb1, sb2)); asm volatile("" : "+s"(r)); return r; };
  auto voff = [&](int e) { int v = (e & 0x7fff) * (DH * 4) + 16 * m; asm volatile("" : "+v"(v)); return v; };
  auto issue = [&](const __amdgpu_buffer_rsrc_t& rs, i32x4 (&x)[3], int vo, unsigned so) {
#pragma unroll
    for (int u = 0; u < 3; ++u) x[u] = ldbuf(rs, vo + 256 * u, so);
  };

  constexpr int T256 = TAU * 256 / 10;
  unsigned tbase;
  auto now = [&]() { return (int)((unsigned)__builtin_amdgcn_s_memrealtime() - tbase); };
  auto grid = [&](int k) { return ((8 * k + w) * (T256 / 8)) >> 8; };
  auto wait_t = [&](int rel) {
    __builtin_amdgcn_sched_barrier(0);
    for (;;) {
      const int d = rel - now();
      if (d <= 0) break;
      if (d > 12) __builtin_amdgcn_s_sleep(2);
    }
    __builtin_amdgcn_sched_barrier(0);
  };
  int go = 0, grel = 0;
  auto gate = [&](int k) {
    const int rel = grid(k);
    __builtin_amdgcn_sched_barrier(0);
    const unsigned long long of = __hip_atomic_load(reinterpret_cast<unsigned long long*>(org), __ATOMIC_RELAXED, __HIP_MEMORY_SCOPE_WORKGROUP);
    int t = now();
    int o = __builtin_amdgcn_readfirstlane((int)of) >> 8;
    const int fl = __builtin_amdgcn_readfirstlane((int)(of >> 32));
    while (o + rel - t > 18) { __builtin_amdgcn_s_sleep(2); o = lds_ld(org) >> 8; t = now(); }
    go = o; grel = rel + GLATE;
    __builtin_amdgcn_sched_barrier(0);
    if (fl) wait_tv<3>(tbase + (unsigned)(o + rel), o + rel - t);
    else wait_tv<0>(tbase + (unsigned)(o + rel), o + rel - t);
    __builtin_amdgcn_sched_barrier(0);
  };
  auto stamp = [&]() {
    __builtin_amdgcn_sched_barrier(0);
    const int o = now() - grel;
    if (o > go) lds_max(org, o << 8);
    __builtin_amdgcn_sched_barrier(0);
  };

  i32x4 xa[3], xb[3], xc[3], xd[3];
  const int s0 = ord == 0x09 ? 2 * NST - RS : NST - (cp == 0 ? RS : 0), s1 = cp == 2 ? NS - 2 * RS : 2 * NST - RS;
  auto vidx_f = [=](int j) {
    int s = j;
    if (j >= NST - RS && j < NST && (cp == 0 || ps == 1)) s += NST;
    if (j >= 2 * NST - RS && j < 2 * NST) s += NST;
    if (cp == 2) { if (j >= NS - 2 * RS) s += j < NS - RS ? RS - (ps == 1 ? 2 : 1) * NST : -RS; }
    else if (j >= NS - RS) s += (cp - 2) * NST;
    return s < l0 ? s + NST : ((unsigned)(s - NST) < (unsigned)l0 ? s - NST : s);
  };
  const int vs0 = l0 ? NST : 0;
  int vperm = 0;
  if (lane < NST) {
    int n = NWAVE * lane + w + SLIP * ((wg >> 1) % SLM), z[2], j[2];
    if (n >= NQ) n -= NQ;
#pragma unroll
    for (int r = 0; r < 2; ++r) { z[r] = (unsigned)((2 * n + r) & 1023) * 1525u >> 16; j[r] = 2 * n + r - NST * z[r]; }
    auto fill = [&](int pos, int add, const int (&hh)[2], const int (&jj)[2]) {
      u32x2 e = {0u, 0u};
#pragma unroll
      for (int r = 0; r < 2; ++r) {
        const int k0 = (int)div3(256 * hh[r] + 1), k = k0 + jj[r] + add;
        e[0] |= (unsigned)k << (16 * r);
        e[1] |= (unsigned)(jj[r] == NST - 1 && 3 * k0 != 256 * hh[r] - 1 ? k + 0x8000 : k + NST) << (16 * r);
      }
      *reinterpret_cast<u32x2*>(tab + pos * 32 + w * 4) = e;
    };
    const int sw = lane < l0 ? NST : 0;
#pragma unroll 1
    for (int p = 0; p < 3; ++p) {
      const int hh[2] = {z[0] + 16 * role(p), z[1] + 16 * role(p)};
      if (p != ps) fill(NST * p + lane - (p == 1 ? sw : 0), lrow(p) * GRP, hh, j);
    }
    if (ps < 3) {
#pragma unroll 1
      for (int ver = 0; ver < 2; ++ver) {
        int hh[2], jj[2];
#pragma unroll
        for (int r = 0; r < 2; ++r) {
          const int sec = j[r] >= 22, x = j[r] - 22 * sec, len = 11 - (ver & sec);
          hh[r] = z[r] + 16 * (ver ^ sec);
          jj[r] = x < len ? x + 11 * ver : x - len + (ver ? 32 : 21 + sec);
        }
        fill((ver ? NS - l0 : NST * ps) + lane + sw, lrow(ps) * GRP, hh, jj);
      }
    }
  }
  asm volatile("" ::: "memory");
  const unsigned tb0 = (unsigned)lds_ld(org + 2);
  int E0 = mytab[0], E1 = mytab[32], E2 = mytab[64];
  const int v0 = voff(E0), v1 = voff(E1);
  asm volatile("" : "+v"(E0), "+v"(E1), "+v"(E2));
  int Ea = 0, Eb = 0, Ec = 0, vo1 = 0;
  auto prologue = [&]() {
    tbase = tb0;
    Ea = E0; Eb = E1; Ec = E2; vo1 = v1;
    __builtin_amdgcn_sched_barrier(0);
    auto puts = [&]() {
      if (w < 6) {
#pragma unroll
        for (int i = 0; i < 4; ++i) put_k(vb + 64 * i, sz[i], ss[i]);
      } else {
#pragma unroll
        for (int i = 0; i < 4; ++i) put_v(vb + 64 * i, sz[i], ss[i]);
      }
      asm volatile("s_waitcnt lgkmcnt(0)" :: "v"(cw) : "memory");
      if (lane == 0) __hip_atomic_fetch_add(rdy, w < 2 ? 0x101 : 0x100, __ATOMIC_RELAXED, __HIP_MEMORY_SCOPE_WORKGROUP);
    };
    if (w < PW) puts();
    if (PWV) asm volatile("s_waitcnt vmcnt(0)" ::: "memory");
    wait_t(ORG0 + grid(0));
    issue(krs, xa, v0, soff(0));
    __builtin_amdgcn_sched_barrier(0);
    if (w >= PW) puts();
    vperm = vidx_f(lane) | (vidx_f(64 + lane) << 8) | ((vidx_f(128 + lane) & 255) << 16);
    asm volatile("" : "+v"(vperm));
  };
  int woff[3];
#pragma unroll
  for (int u = 0; u < 3; ++u) {
    const int qq = 8 * u + (m >> 1);
    woff[u] = qq * 128 + (((2 * g) ^ qq) & 7) * 16 + (m & 1) * 8;
  }
  const int roff = g * 128 + ((2 * (m >> 2) + (m & 1)) ^ g) * 16;
  auto vidx = [&](int j) { return (__builtin_amdgcn_readlane(vperm, j & 63) >> (8 * (j >> 6))) & 255; };

  bf16x8 qf[6];
  u32x4 zs0[3];
  float mrun = -INFINITY;
  int Ep0 = 0x8000, Ep1 = 0x8000;
  auto kzread = [&](u32x4 (&zs)[3], int e) {
    const unsigned* zp = kzs + ((e >> 6) & 0x1ff) * DH + 4 * m;
#pragma unroll
    for (int u = 0; u < 3; ++u) zs[u] = *reinterpret_cast<const u32x4*>(zp + 64 * u);
  };
  auto kdeq = [&](const i32x4 (&x)[3], const u32x4 (&zs)[3], int c) {
#pragma unroll
    for (int u = 0; u < 3; ++u) {
      const i32x4 y = x[u];
      u32x2 o = {deq2(y[0], y[1], zs[u][0], zs[u][2]), deq2(y[2], y[3], zs[u][1], zs[u][3])};
      *reinterpret_cast<u32x2*>(myst + (woff[u] ^ (c << 4))) = o;
    }
  };
  auto kread = [&](u32x4 (&a)[6]) {
#pragma unroll
    for (int j = 0; j < 6; ++j) a[j] = *reinterpret_cast<const u32x4*>(myst + ((roff + 512 * j) ^ ((j & 1) << 6)));
  };
  auto kfin = [&](const u32x4 (&a)[6], int e0, int e1, int i) {
    f32x4 acc = {0.f, 0.f, 0.f, 0.f};
#pragma unroll
    for (int j = 0; j < 6; ++j) acc = __builtin_amdgcn_mfma_f32_16x16x32_bf16(__builtin_bit_cast(bf16x8, a[j]), qf[j], acc, 0, 0, 0);
    const unsigned o = cvtpk(bfround(acc[0]) * scale, bfround(acc[1]) * scale);
    if ((unsigned)e0 - mb < msz) { mrun = fmaxf(mrun, bflo(o)); mypl[i * 16] = (unsigned short)o; }
    if ((unsigned)e1 - mb < msz) { mrun = fmaxf(mrun, bfhi(o)); mypl[i * 16 + 16] = (unsigned short)(o >> 16); }
  };
  auto kissue = [&](i32x4 (&x)[3], int vo, unsigned so) { issue(krs, x, vo, so); stamp(); };
#pragma unroll
  for (int j = 0; j < 6; ++j) asm volatile("" : "=v"(qf[j]));
#pragma unroll
  for (int u = 0; u < 3; ++u) asm volatile("" : "=v"(zs0[u]));
  auto adopt = [&]() {
    int x = 0;
    for (int spin = 0; spin < (1 << 22) && !(x = lds_ld(xfl)); ++spin) __builtin_amdgcn_s_sleep(1);
    asm volatile("" ::: "memory");
    if ((x & 2) && mseg == ps) { mrun = xmax[4 * w + (m & 3)]; stats[(ps * NWAVE + w) * 8 + (m & 3)] = mrun; }
  };
  static_assert(LD % 2 == 0 && LD > 6 && LD <= NST - RS, "");
  const unsigned long long spec = 1ull | (1ull << (QI / 2)) | (1ull << (ps ? 8 : LD / 2 - 2)) | (ps == 0 ? (1ull << (LD / 2 + 23)) | (1ull << 60) : 0ull) | (ps == 1 ? 1ull << 44 : 0ull);
  prologue();
  {
#pragma unroll 1
    for (int i = 0; i < NS - 1; i += 2) {
      const unsigned so1 = soff(i + 1), so2 = soff(i + 2);
      gate(i + 1);
      kissue(xb, vo1, so1);
      if ((spec >> (i >> 1)) & 1) {
        if (i < 40) {
          const int sh = i > QI ? 8 : 0, need = i > QI ? NWAVE + 1 : (i == QI ? 6 : 2), mk = i < QI ? 3 : 255;
          for (int spin = 0; spin < (1 << 22) && ((lds_ld(rdy) >> sh) & mk) < need; ++spin) __builtin_amdgcn_s_sleep(1);
          asm volatile("" ::: "memory");
          if (i == 0) {
            kzread(zs0, Ea);
          }
          if (i == QI) {
#pragma unroll
            for (int j = 0; j < 6; ++j)
              qf[j] = *reinterpret_cast<const bf16x8*>(qs + min(m, 3 * SQ - 1) * DH + 32 * j + 8 * g);
          }
        } else if (i < 100) {
          mrun = rows<true>(mrun);
          if (lane < 2 * SQ) stats[(mseg * NWAVE + w) * 8 + (m & 3)] = mrun;
          lds_add(rdy, 1 << 24);
        } else adopt();
      }
      u32x4 a[6], zs1[3];
      kread(a);
      kzread(zs1, Eb);
      const int in = i + 3 < NS ? i + 3 : vs0;
      const int En0 = mytab[in * 32], En1 = mytab[in * 32 + 32];
      kdeq(xa, zs0, 0);
      kfin(a, Ep0, Ep1, i - 2);
      const int vo2 = voff(Ec);
      gate(i + 2);
      kissue(xa, vo2, so2);
      kzread(zs0, Ec);
      kdeq(xb, zs1, 1);
      lds_add(pprog, 1);
      Ep0 = Ea; Ep1 = Eb; Ea = Ec; Eb = En0; Ec = En1;
      vo1 = voff(Eb);
    }
  }
  f32x4 oacc[3][4];
  float srun;
  unsigned z0;
  auto vdeq = [&](const i32x4& y, unsigned zs, float (&f)[4]) {
    const float z = bflo(zs), s = bfhi(zs);
    const unsigned t0 = cvtpk((float)y[0] - z, (float)y[1] - z), t1 = cvtpk((float)y[2] - z, (float)y[3] - z);
    f[0] = bflo(t0) * s; f[1] = bfhi(t0) * s; f[2] = bflo(t1) * s; f[3] = bfhi(t1) * s;
  };
  auto prob = [&](int e, unsigned lg) { return __expf(__uint_as_float((unsigned)e - mb < msz ? lg << 16 : 0xff800000u) - mrun); };
  {
    gate(NS);
    issue(vrs, xb, vo1, sb0);
    stamp();
    u32x4 a[6];
    kread(a);
    const int En0 = mytab[vs0 * 32 + 64], En1 = mytab[vs0 * 32 + 96];
    const unsigned zv = vzs[Eb & 0x7fff], lg = mypl[vs0 * 16];
    z0 = vzs[Ec & 0x7fff];
    kdeq(xa, zs0, 0);
    kfin(a, Ep0, Ep1, NS - 3);
    const int vo2 = voff(Ec);
    gate(NS + 1);
    issue(vrs, xa, vo2, sb0);
    stamp();
    kread(a);
    unsigned bfr[3][4];
#pragma unroll
    for (int db = 0; db < 3; ++db) {
      float f[4];
      vdeq(xb[db], zv, f);
#pragma unroll
      for (int c = 0; c < 4; ++c) bfr[db][c] = cvtpk(f[c], 0.f);
      if (db == 0) { asm volatile("" : "+v"(bfr[0][0]), "+v"(bfr[0][1]), "+v"(bfr[0][2]), "+v"(bfr[0][3])); }
    }
    kfin(a, Ea, 0x8000, NS - 1);
    mrun = rows<true>(mrun);
    if (lane < 3 * SQ) stats[(mseg * NWAVE + w) * 8 + (m & 3)] = mrun;
    lds_add(rdy, 0x10000);
    if (mseg == 3) mrun = 0.f;
    srun = prob(Eb, lg);
    const u32x2 pa = {cvtpk(srun, 0.f), 0u};
#pragma unroll
    for (int db = 0; db < 3; ++db)
#pragma unroll
      for (int c = 0; c < 4; ++c)
        oacc[db][c] = __builtin_amdgcn_mfma_f32_16x16x16bf16_1k(__builtin_bit_cast(bf16x4, pa), __builtin_bit_cast(bf16x4, u32x2{bfr[db][c], 0u}), f32x4{0.f, 0.f, 0.f, 0.f}, 0, 0, 0);
    lds_add(pprog, 2);
    Ea = Ec; Eb = En0; Ec = En1;
    vo1 = voff(Eb);
  }

  auto vend = [&](int p, int k) {
    const int pm = (ord >> (2 * p)) & 3;
    float* oslot = reinterpret_cast<float*>(p & 1 ? slot1 + w * OSL : myst);
    if (g == pm) {
#pragma unroll
      for (int db = 0; db < 3; ++db)
        if (k >> db & 1) {
#pragma unroll
          for (int r = 0; r < 4; ++r)
            *reinterpret_cast<f32x4*>(oslot + r * DH + 64 * db + 4 * m) = f32x4{oacc[db][0][r], oacc[db][1][r], oacc[db][2][r], oacc[db][3][r]};
          asm volatile("" ::: "memory");
        }
    }
    if (k & 8) {
      const float sr = rows<false>(srun);
      if (g == pm && mseg == pm) stats[(pm * NWAVE + w) * 8 + 4 + (m & 3)] = sr;
      lds_add(cnt + p, 1 << w);
    }
  };
  int El = 0, Ia = vs0 + 1, Ib = vs0 + 2, Ic = vs0 + 3;
  unsigned so1 = sb0;
  auto vpair = [&](auto last, int j) {
    constexpr bool LAST = decltype(last)::value;
    gate(NS + j + 1);
    issue(vrs, xb, vo1, so1);
    stamp();
    const unsigned z1 = vzs[Eb & 0x7fff], lg0 = mypl[Ia * 16], lg1 = mypl[Ib * 16];
    const unsigned so2 = soff(Ic);
    Ia = Ic; Ib = vidx(j + 3); Ic = vidx(j + 4); so1 = soff(Ib);
    const int En0 = mytab[Ib * 32], En1 = mytab[Ic * 32];
    if constexpr (LAST) El = mytab[vidx(j + 5) * 32];
    float f[3][4];
#pragma unroll
    for (int db = 0; db < 3; ++db) {
      vdeq(xa[db], z0, f[db]);
      asm volatile("" : "+v"(f[db][0]), "+v"(f[db][1]), "+v"(f[db][2]), "+v"(f[db][3]));
    }
    const float p0 = prob(Ea, lg0), p1 = prob(Eb, lg1);
    srun += p0 + p1;
    u32x2 pa = {cvtpk(p0, p1), 0u};
    asm volatile("" : "+v"(pa[0]), "+v"(srun));
    const int vo2 = voff(Ec);
    int vn[3];
    if constexpr (LAST) { vn[0] = voff(En0); vn[1] = voff(En1); vn[2] = voff(El); }
    gate(NS + j + 2);
    issue(vrs, xa, vo2, so2);
    if constexpr (LAST) __builtin_amdgcn_sched_barrier(0); else stamp();
    z0 = vzs[Ec & 0x7fff];
    unsigned bfr[3][4];
#pragma unroll
    for (int db = 0; db < 3; ++db) {
      float y[4];
      vdeq(xb[db], z1, y);
#pragma unroll
      for (int c = 0; c < 4; ++c) bfr[db][c] = cvtpk(f[db][c], y[c]);
      if (!LAST && db == 0) { asm volatile("" : "+v"(bfr[0][0]), "+v"(bfr[0][1]), "+v"(bfr[0][2]), "+v"(bfr[0][3])); }
    }
#pragma unroll
    for (int db = 0; db < 3; ++db)
#pragma unroll
      for (int c = 0; c < 4; ++c)
        oacc[db][c] = __builtin_amdgcn_mfma_f32_16x16x16bf16_1k(__builtin_bit_cast(bf16x4, pa), __builtin_bit_cast(bf16x4, u32x2{bfr[db][c], 0u}), oacc[db][c], 0, 0, 0);
    Ea = Ec; Eb = En0; Ec = En1;
    if constexpr (!LAST) vo1 = voff(Eb);
    else {
      const int tr = go + grel - GLATE;
      wait_t(tr + SD);
      issue(vrs, xc, vn[0], so2);
      wait_t(tr + 2 * SD);
      issue(vrs, xd, vn[1], so2);
      wait_t(tr + 3 * SD);
      issue(vrs, xb, vn[2], so2);
      __builtin_amdgcn_sched_barrier(0);
    }
  };
#pragma unroll 1
  for (int j = 1; j < NS - 6; j += 2) {
    vpair(IC<0>{}, j);
    lds_add(pprog, 1);
    const unsigned d0 = j + 2 - (s0 | 1), d1 = j + 2 - (s1 | 1);
    if (d0 <= 4u || d1 <= 4u) vend(d0 > 4u, ((d0 > 4u && cp == 2 ? 0x0c3 : 0xc21) >> (2 * min(d0, d1))) & 15);
    if (j == (ps < 3 ? 42 * ps - 13 : 0)) adopt();
    if (j == (cp == 2 ? 99 : 71)) {
      for (int spin = 0; spin < (1 << 22) && lds_ld(mrg) < 1; ++spin) __builtin_amdgcn_s_sleep(1);
      asm volatile("" ::: "memory");

      if (mseg == 3) {
        mrun = -INFINITY;
#pragma unroll
        for (int ww = 0; ww < NWAVE; ++ww) mrun = fmaxf(mrun, stats[(cp * NWAVE + ww) * 8 + (m & 3)]);
      }
    }
  }
  static_assert(RS == 6, "");
  vend(2, 15);
  mb = frow(cp) * GRP; msz = mseg == 3 ? NROW * GRP : 0;
  vpair(IC<1>{}, NS - 6);
  float* oslot = reinterpret_cast<float*>(slot1 + w * OSL);
  u32x2 pa;
  unsigned z1, z2, z3;
  {
    const int I = Ia;
    z1 = vzs[Eb & 0x7fff]; z2 = vzs[Ec & 0x7fff]; z3 = vzs[El & 0x7fff];
    const unsigned lg0 = mypl[I * 16], lg1 = mypl[I * 16 + 16], lg2 = mypl[I * 16 + 32], lg3 = mypl[I * 16 + 48];
    const float p0 = prob(Ea, lg0), p1 = prob(Eb, lg1), p2 = prob(Ec, lg2), p3 = prob(El, lg3);
    srun += p0 + p1;
    srun += p2 + p3;
    pa = u32x2{cvtpk(p0, p1), cvtpk(p2, p3)};
    const float sr = rows<false>(srun);
    if (lane >= 60) stats[(3 * NWAVE + w) * 8 + 4 + (m & 3)] = sr;
    lds_add(cnt + 3, 0x100 << w);
    for (int spin = 0; spin < (1 << 22) && lds_ld(mrg) < 2; ++spin) __builtin_amdgcn_s_sleep(1);
    asm volatile("" : "+v"(pa), "+v"(z1), "+v"(z2), "+v"(z3) :: "memory");
  }
  __builtin_amdgcn_sched_barrier(0);
  unsigned b01[3][4];
  float fd[3][4];
#pragma unroll
  for (int db = 0; db < 3; ++db) vdeq(xa[db], z0, fd[db]);
#pragma unroll
  for (int db = 0; db < 3; ++db) {
    float y[4];
    vdeq(xc[db], z1, y);
#pragma unroll
    for (int c = 0; c < 4; ++c) b01[db][c] = cvtpk(fd[db][c], y[c]);
  }
#pragma unroll
  for (int db = 0; db < 3; ++db) {
    vdeq(xd[db], z2, fd[db]);
    asm volatile("" : "+v"(fd[db][0]), "+v"(fd[db][1]), "+v"(fd[db][2]), "+v"(fd[db][3]), "+v"(b01[db][0]), "+v"(b01[db][1]), "+v"(b01[db][2]), "+v"(b01[db][3]));
  }
  __builtin_amdgcn_sched_barrier(0);
#pragma unroll
  for (int db = 0; db < 3; ++db) {
    float y[4];
    vdeq(xb[db], z3, y);
#pragma unroll
    for (int c = 0; c < 4; ++c)
      oacc[db][c] = __builtin_amdgcn_mfma_f32_16x16x16bf16_1k(__builtin_bit_cast(bf16x4, pa), __builtin_bit_cast(bf16x4, u32x2{b01[db][c], cvtpk(fd[db][c], y[c])}), oacc[db][c], 0, 0, 0);
    if (g == 3) {
#pragma unroll
      for (int r = 0; r < 4; ++r)
        *reinterpret_cast<f32x4*>(oslot + r * DH + 64 * db + 4 * m) = f32x4{oacc[db][0][r], oacc[db][1][r], oacc[db][2][r], oacc[db][3][r]};
    }
    __builtin_amdgcn_sched_barrier(0);
  }
  lds_add(cnt + 3, 1 << w);
}

}

extern "C" void kernel_launch(void* const* d_in, const int* in_sizes, int n_in,
                              void* d_out, int out_size, void* d_ws, size_t ws_size,
                              hipStream_t stream) {
  const __hip_bfloat16* q  = (const __hip_bfloat16*)d_in[0];
  const int* qk            = (const int*)d_in[1];
  const int* qv            = (const int*)d_in[2];
  const __hip_bfloat16* ks = (const __hip_bfloat16*)d_in[3];
  const __hip_bfloat16* kz = (const __hip_bfloat16*)d_in[4];
  const __hip_bfloat16* vs = (const __hip_bfloat16*)d_in[5];
  const __hip_bfloat16* vz = (const __hip_bfloat16*)d_in[6];
  kivi_attn_kernel<<<NWG, NTHR + 64, 0, stream>>>(q, qk, qv, ks, kz, vs, vz, (__hip_bfloat16*)d_out, (unsigned char*)d_ws);
}
```
